```python
import torch

def _softmax(x, axis=-1):
    return torch.softmax(x, dim=axis)
def _gelu(x):
    return torch.nn.functional.gelu(x, approximate="tanh")
def _silu(x):
    return torch.nn.functional.silu(x)
def _sigmoid(x):
    return torch.sigmoid(x)
def _layernorm(x, axis=-1, eps=1e-5):
    m = x.mean(dim=axis, keepdim=True)
    v = x.var(dim=axis, keepdim=True, unbiased=False)
    return (x - m) / torch.sqrt(v + eps)
def _scatter_add(tgt, idx, src, axis=0):
    return tgt.clone().index_add_(axis, idx, src)
def _segment_sum(src, seg, n):
    out = torch.zeros((n,) + src.shape[1:], dtype=src.dtype)
    return out.index_add_(0, seg, src)
def _pad(x, pw):
    flat = tuple(v for pair in reversed(pw) for v in pair)
    return torch.nn.functional.pad(x, flat)

kernel_name = 'interleave_layers__spectral_gfnet__glu_gated_swiglu__prompt'

def setup_inputs():

    _w = (lambda a: (lambda t: t.to(torch.bfloat16) if t.is_floating_point() else t)(torch.as_tensor(a)))
    def _A_setup():
        B, S, D = 1, 2048, 640
        Df = D // 2 + 1
        return {
            "x": _w((lambda *s: torch.randn(*s, dtype=torch.bfloat16) * 0.05)(B, S, D)),
            "w_r": _w((lambda *s: torch.randn(*s, dtype=torch.bfloat16) * 0.4)(S, Df) * 0.02),
            "w_i": _w((lambda *s: torch.randn(*s, dtype=torch.bfloat16) * 0.4)(S, Df) * 0.02),
        }
    _A_in = _A_setup()

    def _B_setup():
        B, S, D, F = 1, 2048, 640, 1728
        return {
            "x": _w((lambda *s: torch.randn(*s, dtype=torch.bfloat16) * 0.05)(B, S, D)),
            "w1": _w((lambda *s: torch.randn(*s, dtype=torch.bfloat16) * 0.2)(D, F) * (2.0 / D) ** 0.5),
            "w3": _w((lambda *s: torch.randn(*s, dtype=torch.bfloat16) * 0.2)(D, F) * (2.0 / D) ** 0.5),
            "w2": _w((lambda *s: torch.randn(*s, dtype=torch.bfloat16) * 0.2)(F, D) * (2.0 / F) ** 0.5),
        }
    _B_in = _B_setup()

    return {
        "A_x": _A_in["x"],
        "A_w_r": _A_in["w_r"],
        "A_w_i": _A_in["w_i"],
        "B_x": _B_in["x"],
        "B_w1": _B_in["w1"],
        "B_w3": _B_in["w3"],
        "B_w2": _B_in["w2"],
    }

def reference(A_x, A_w_r, A_w_i, B_x, B_w1, B_w3, B_w2):
    def _A_ref(x, w_r, w_i):
        B, S, D = x.shape
        X = (lambda x, axes=(-2,-1): torch.fft.rfft2(x.float(), dim=axes))(x, axes=(-2, -1))
        W = w_r + 1j * w_i
        X = X * W
        y = (lambda x, s, axes=(-2,-1): torch.fft.irfft2(x, s=s, dim=axes).to(torch.bfloat16))(X, (S, D), axes=(-2, -1))
        return y

    def _B_ref(x, w1, w3, w2):
        gate = torch.einsum("bsd,df->bsf", x, w1)
        up = torch.einsum("bsd,df->bsf", x, w3)
        h = _silu(gate) * up
        out = torch.einsum("bsf,fd->bsd", h, w2)
        return out

    x = A_x
    x = x + _A_ref(_layernorm(x, axis=-1), A_w_r, A_w_i)
    x = x + _B_ref(_layernorm(x, axis=-1), B_w1, B_w3, B_w2)
    x = x + _A_ref(_layernorm(x, axis=-1), A_w_r, A_w_i)
    x = x + _B_ref(_layernorm(x, axis=-1), B_w1, B_w3, B_w2)
    return x
```

```cpp
#include <hip/hip_runtime.h>
#include <hip/hip_bf16.h>
#include <stdint.h>
__device__ const float2 TW640[640] = {
{0x1.0000000000000p+0,-0x0.0p+0},{0x1.fff9ae0000000p-1,-0x1.41b1a40000000p-7},{0x1.ffe6bc0000000p-1,-0x1.41adac0000000p-6},{0x1.ffc7280000000p-1,-0x1.e27a980000000p-6},
{0x1.ff9af20000000p-1,-0x1.419dce0000000p-5},{0x1.ff621e0000000p-1,-0x1.91f6600000000p-5},{0x1.ff1cac0000000p-1,-0x1.e245060000000p-5},{0x1.feca9c0000000p-1,-0x1.1943e40000000p-4},
{0x1.fe6bf20000000p-1,-0x1.415e540000000p-4},{0x1.fe00b20000000p-1,-0x1.6970d40000000p-4},{0x1.fd88da0000000p-1,-0x1.917a6c0000000p-4},{0x1.fd04700000000p-1,-0x1.b97a1a0000000p-4},
{0x1.fc73780000000p-1,-0x1.e16ee40000000p-4},{0x1.fbd5f40000000p-1,-0x1.04abe80000000p-3},{0x1.fb2be80000000p-1,-0x1.1899ee0000000p-3},{0x1.fa75580000000p-1,-0x1.2c81060000000p-3},
{0x1.f9b24a0000000p-1,-0x1.4060b60000000p-3},{0x1.f8e2c00000000p-1,-0x1.54387e0000000p-3},{0x1.f806c20000000p-1,-0x1.6807e20000000p-3},{0x1.f71e540000000p-1,-0x1.7bce620000000p-3},
{0x1.f6297c0000000p-1,-0x1.8f8b840000000p-3},{0x1.f528420000000p-1,-0x1.a33eca0000000p-3},{0x1.f41aa80000000p-1,-0x1.b6e7b80000000p-3},{0x1.f300b80000000p-1,-0x1.ca85d20000000p-3},
{0x1.f1da780000000p-1,-0x1.de189a0000000p-3},{0x1.f0a7f00000000p-1,-0x1.f19f980000000p-3},{0x1.ef69260000000p-1,-0x1.028d260000000p-2},{0x1.ee1e220000000p-1,-0x1.0c44200000000p-2},
{0x1.ecc6ee0000000p-1,-0x1.15f47c0000000p-2},{0x1.eb63920000000p-1,-0x1.1f9dfc0000000p-2},{0x1.e9f4160000000p-1,-0x1.2940620000000p-2},{0x1.e878820000000p-1,-0x1.32db740000000p-2},
{0x1.e6f0e20000000p-1,-0x1.3c6ef40000000p-2},{0x1.e55d3c0000000p-1,-0x1.45faa40000000p-2},{0x1.e3bd9e0000000p-1,-0x1.4f7e4a0000000p-2},{0x1.e212100000000p-1,-0x1.58f9a80000000p-2},
{0x1.e05a9e0000000p-1,-0x1.626c820000000p-2},{0x1.de97500000000p-1,-0x1.6bd69e0000000p-2},{0x1.dcc8340000000p-1,-0x1.7537c20000000p-2},{0x1.daed540000000p-1,-0x1.7e8fae0000000p-2},
{0x1.d906bc0000000p-1,-0x1.87de2a0000000p-2},{0x1.d7147a0000000p-1,-0x1.9122fc0000000p-2},{0x1.d516960000000p-1,-0x1.9a5de60000000p-2},{0x1.d30d200000000p-1,-0x1.a38eb20000000p-2},
{0x1.d0f8240000000p-1,-0x1.acb5240000000p-2},{0x1.ced7b00000000p-1,-0x1.b5d1000000000p-2},{0x1.ccabce0000000p-1,-0x1.bee2100000000p-2},{0x1.ca74900000000p-1,-0x1.c7e81a0000000p-2},
{0x1.c832020000000p-1,-0x1.d0e2e20000000p-2},{0x1.c5e4320000000p-1,-0x1.d9d2340000000p-2},{0x1.c38b300000000p-1,-0x1.e2b5d40000000p-2},{0x1.c127080000000p-1,-0x1.eb8d8a0000000p-2},
{0x1.beb7cc0000000p-1,-0x1.f459200000000p-2},{0x1.bc3d8a0000000p-1,-0x1.fd185e0000000p-2},{0x1.b9b8520000000p-1,-0x1.02e5860000000p-1},{0x1.b728340000000p-1,-0x1.07387a0000000p-1},
{0x1.b48d400000000p-1,-0x1.0b84ee0000000p-1},{0x1.b1e7880000000p-1,-0x1.0fcaca0000000p-1},{0x1.af37180000000p-1,-0x1.1409f00000000p-1},{0x1.ac7c060000000p-1,-0x1.1842480000000p-1},
{0x1.a9b6620000000p-1,-0x1.1c73b40000000p-1},{0x1.a6e63e0000000p-1,-0x1.209e1c0000000p-1},{0x1.a40ba80000000p-1,-0x1.24c1640000000p-1},{0x1.a126b60000000p-1,-0x1.28dd740000000p-1},
{0x1.9e377a0000000p-1,-0x1.2cf2300000000p-1},{0x1.9b3e040000000p-1,-0x1.30ff800000000p-1},{0x1.983a6a0000000p-1,-0x1.3505480000000p-1},{0x1.952cbc0000000p-1,-0x1.3903720000000p-1},
{0x1.9215100000000p-1,-0x1.3cf9e20000000p-1},{0x1.8ef3780000000p-1,-0x1.40e8800000000p-1},{0x1.8bc8060000000p-1,-0x1.44cf320000000p-1},{0x1.8892d20000000p-1,-0x1.48ade20000000p-1},
{0x1.8553ee0000000p-1,-0x1.4c84740000000p-1},{0x1.820b700000000p-1,-0x1.5052d40000000p-1},{0x1.7eb96a0000000p-1,-0x1.5418e60000000p-1},{0x1.7b5df20000000p-1,-0x1.57d6940000000p-1},
{0x1.77f91e0000000p-1,-0x1.5b8bc60000000p-1},{0x1.748b040000000p-1,-0x1.5f38640000000p-1},{0x1.7113b80000000p-1,-0x1.62dc580000000p-1},{0x1.6d93520000000p-1,-0x1.66778c0000000p-1},
{0x1.6a09e60000000p-1,-0x1.6a09e60000000p-1},{0x1.66778c0000000p-1,-0x1.6d93520000000p-1},{0x1.62dc580000000p-1,-0x1.7113b80000000p-1},{0x1.5f38640000000p-1,-0x1.748b040000000p-1},
{0x1.5b8bc60000000p-1,-0x1.77f91e0000000p-1},{0x1.57d6940000000p-1,-0x1.7b5df20000000p-1},{0x1.5418e60000000p-1,-0x1.7eb96a0000000p-1},{0x1.5052d40000000p-1,-0x1.820b700000000p-1},
{0x1.4c84740000000p-1,-0x1.8553ee0000000p-1},{0x1.48ade20000000p-1,-0x1.8892d20000000p-1},{0x1.44cf320000000p-1,-0x1.8bc8060000000p-1},{0x1.40e8800000000p-1,-0x1.8ef3780000000p-1},
{0x1.3cf9e20000000p-1,-0x1.9215100000000p-1},{0x1.3903720000000p-1,-0x1.952cbc0000000p-1},{0x1.3505480000000p-1,-0x1.983a6a0000000p-1},{0x1.30ff800000000p-1,-0x1.9b3e040000000p-1},
{0x1.2cf2300000000p-1,-0x1.9e377a0000000p-1},{0x1.28dd740000000p-1,-0x1.a126b60000000p-1},{0x1.24c1640000000p-1,-0x1.a40ba80000000p-1},{0x1.209e1c0000000p-1,-0x1.a6e63e0000000p-1},
{0x1.1c73b40000000p-1,-0x1.a9b6620000000p-1},{0x1.1842480000000p-1,-0x1.ac7c060000000p-1},{0x1.1409f00000000p-1,-0x1.af37180000000p-1},{0x1.0fcaca0000000p-1,-0x1.b1e7880000000p-1},
{0x1.0b84ee0000000p-1,-0x1.b48d400000000p-1},{0x1.07387a0000000p-1,-0x1.b728340000000p-1},{0x1.02e5860000000p-1,-0x1.b9b8520000000p-1},{0x1.fd185e0000000p-2,-0x1.bc3d8a0000000p-1},
{0x1.f459200000000p-2,-0x1.beb7cc0000000p-1},{0x1.eb8d8a0000000p-2,-0x1.c127080000000p-1},{0x1.e2b5d40000000p-2,-0x1.c38b300000000p-1},{0x1.d9d2340000000p-2,-0x1.c5e4320000000p-1},
{0x1.d0e2e20000000p-2,-0x1.c832020000000p-1},{0x1.c7e81a0000000p-2,-0x1.ca74900000000p-1},{0x1.bee2100000000p-2,-0x1.ccabce0000000p-1},{0x1.b5d1000000000p-2,-0x1.ced7b00000000p-1},
{0x1.acb5240000000p-2,-0x1.d0f8240000000p-1},{0x1.a38eb20000000p-2,-0x1.d30d200000000p-1},{0x1.9a5de60000000p-2,-0x1.d516960000000p-1},{0x1.9122fc0000000p-2,-0x1.d7147a0000000p-1},
{0x1.87de2a0000000p-2,-0x1.d906bc0000000p-1},{0x1.7e8fae0000000p-2,-0x1.daed540000000p-1},{0x1.7537c20000000p-2,-0x1.dcc8340000000p-1},{0x1.6bd69e0000000p-2,-0x1.de97500000000p-1},
{0x1.626c820000000p-2,-0x1.e05a9e0000000p-1},{0x1.58f9a80000000p-2,-0x1.e212100000000p-1},{0x1.4f7e4a0000000p-2,-0x1.e3bd9e0000000p-1},{0x1.45faa40000000p-2,-0x1.e55d3c0000000p-1},
{0x1.3c6ef40000000p-2,-0x1.e6f0e20000000p-1},{0x1.32db740000000p-2,-0x1.e878820000000p-1},{0x1.2940620000000p-2,-0x1.e9f4160000000p-1},{0x1.1f9dfc0000000p-2,-0x1.eb63920000000p-1},
{0x1.15f47c0000000p-2,-0x1.ecc6ee0000000p-1},{0x1.0c44200000000p-2,-0x1.ee1e220000000p-1},{0x1.028d260000000p-2,-0x1.ef69260000000p-1},{0x1.f19f980000000p-3,-0x1.f0a7f00000000p-1},
{0x1.de189a0000000p-3,-0x1.f1da780000000p-1},{0x1.ca85d20000000p-3,-0x1.f300b80000000p-1},{0x1.b6e7b80000000p-3,-0x1.f41aa80000000p-1},{0x1.a33eca0000000p-3,-0x1.f528420000000p-1},
{0x1.8f8b840000000p-3,-0x1.f6297c0000000p-1},{0x1.7bce620000000p-3,-0x1.f71e540000000p-1},{0x1.6807e20000000p-3,-0x1.f806c20000000p-1},{0x1.54387e0000000p-3,-0x1.f8e2c00000000p-1},
{0x1.4060b60000000p-3,-0x1.f9b24a0000000p-1},{0x1.2c81060000000p-3,-0x1.fa75580000000p-1},{0x1.1899ee0000000p-3,-0x1.fb2be80000000p-1},{0x1.04abe80000000p-3,-0x1.fbd5f40000000p-1},
{0x1.e16ee40000000p-4,-0x1.fc73780000000p-1},{0x1.b97a1a0000000p-4,-0x1.fd04700000000p-1},{0x1.917a6c0000000p-4,-0x1.fd88da0000000p-1},{0x1.6970d40000000p-4,-0x1.fe00b20000000p-1},
{0x1.415e540000000p-4,-0x1.fe6bf20000000p-1},{0x1.1943e40000000p-4,-0x1.feca9c0000000p-1},{0x1.e245060000000p-5,-0x1.ff1cac0000000p-1},{0x1.91f6600000000p-5,-0x1.ff621e0000000p-1},
{0x1.419dce0000000p-5,-0x1.ff9af20000000p-1},{0x1.e27a980000000p-6,-0x1.ffc7280000000p-1},{0x1.41adac0000000p-6,-0x1.ffe6bc0000000p-1},{0x1.41b1a40000000p-7,-0x1.fff9ae0000000p-1},
{0x1.1a62640000000p-54,-0x1.0000000000000p+0},{-0x1.41b1a40000000p-7,-0x1.fff9ae0000000p-1},{-0x1.41adac0000000p-6,-0x1.ffe6bc0000000p-1},{-0x1.e27a980000000p-6,-0x1.ffc7280000000p-1},
{-0x1.419dce0000000p-5,-0x1.ff9af20000000p-1},{-0x1.91f6600000000p-5,-0x1.ff621e0000000p-1},{-0x1.e245060000000p-5,-0x1.ff1cac0000000p-1},{-0x1.1943e40000000p-4,-0x1.feca9c0000000p-1},
{-0x1.415e540000000p-4,-0x1.fe6bf20000000p-1},{-0x1.6970d40000000p-4,-0x1.fe00b20000000p-1},{-0x1.917a6c0000000p-4,-0x1.fd88da0000000p-1},{-0x1.b97a1a0000000p-4,-0x1.fd04700000000p-1},
{-0x1.e16ee40000000p-4,-0x1.fc73780000000p-1},{-0x1.04abe80000000p-3,-0x1.fbd5f40000000p-1},{-0x1.1899ee0000000p-3,-0x1.fb2be80000000p-1},{-0x1.2c81060000000p-3,-0x1.fa75580000000p-1},
{-0x1.4060b60000000p-3,-0x1.f9b24a0000000p-1},{-0x1.54387e0000000p-3,-0x1.f8e2c00000000p-1},{-0x1.6807e20000000p-3,-0x1.f806c20000000p-1},{-0x1.7bce620000000p-3,-0x1.f71e540000000p-1},
{-0x1.8f8b840000000p-3,-0x1.f6297c0000000p-1},{-0x1.a33eca0000000p-3,-0x1.f528420000000p-1},{-0x1.b6e7b80000000p-3,-0x1.f41aa80000000p-1},{-0x1.ca85d20000000p-3,-0x1.f300b80000000p-1},
{-0x1.de189a0000000p-3,-0x1.f1da780000000p-1},{-0x1.f19f980000000p-3,-0x1.f0a7f00000000p-1},{-0x1.028d260000000p-2,-0x1.ef69260000000p-1},{-0x1.0c44200000000p-2,-0x1.ee1e220000000p-1},
{-0x1.15f47c0000000p-2,-0x1.ecc6ee0000000p-1},{-0x1.1f9dfc0000000p-2,-0x1.eb63920000000p-1},{-0x1.2940620000000p-2,-0x1.e9f4160000000p-1},{-0x1.32db740000000p-2,-0x1.e878820000000p-1},
{-0x1.3c6ef40000000p-2,-0x1.e6f0e20000000p-1},{-0x1.45faa40000000p-2,-0x1.e55d3c0000000p-1},{-0x1.4f7e4a0000000p-2,-0x1.e3bd9e0000000p-1},{-0x1.58f9a80000000p-2,-0x1.e212100000000p-1},
{-0x1.626c820000000p-2,-0x1.e05a9e0000000p-1},{-0x1.6bd69e0000000p-2,-0x1.de97500000000p-1},{-0x1.7537c20000000p-2,-0x1.dcc8340000000p-1},{-0x1.7e8fae0000000p-2,-0x1.daed540000000p-1},
{-0x1.87de2a0000000p-2,-0x1.d906bc0000000p-1},{-0x1.9122fc0000000p-2,-0x1.d7147a0000000p-1},{-0x1.9a5de60000000p-2,-0x1.d516960000000p-1},{-0x1.a38eb20000000p-2,-0x1.d30d200000000p-1},
{-0x1.acb5240000000p-2,-0x1.d0f8240000000p-1},{-0x1.b5d1000000000p-2,-0x1.ced7b00000000p-1},{-0x1.bee2100000000p-2,-0x1.ccabce0000000p-1},{-0x1.c7e81a0000000p-2,-0x1.ca74900000000p-1},
{-0x1.d0e2e20000000p-2,-0x1.c832020000000p-1},{-0x1.d9d2340000000p-2,-0x1.c5e4320000000p-1},{-0x1.e2b5d40000000p-2,-0x1.c38b300000000p-1},{-0x1.eb8d8a0000000p-2,-0x1.c127080000000p-1},
{-0x1.f459200000000p-2,-0x1.beb7cc0000000p-1},{-0x1.fd185e0000000p-2,-0x1.bc3d8a0000000p-1},{-0x1.02e5860000000p-1,-0x1.b9b8520000000p-1},{-0x1.07387a0000000p-1,-0x1.b728340000000p-1},
{-0x1.0b84ee0000000p-1,-0x1.b48d400000000p-1},{-0x1.0fcaca0000000p-1,-0x1.b1e7880000000p-1},{-0x1.1409f00000000p-1,-0x1.af37180000000p-1},{-0x1.1842480000000p-1,-0x1.ac7c060000000p-1},
{-0x1.1c73b40000000p-1,-0x1.a9b6620000000p-1},{-0x1.209e1c0000000p-1,-0x1.a6e63e0000000p-1},{-0x1.24c1640000000p-1,-0x1.a40ba80000000p-1},{-0x1.28dd740000000p-1,-0x1.a126b60000000p-1},
{-0x1.2cf2300000000p-1,-0x1.9e377a0000000p-1},{-0x1.30ff800000000p-1,-0x1.9b3e040000000p-1},{-0x1.3505480000000p-1,-0x1.983a6a0000000p-1},{-0x1.3903720000000p-1,-0x1.952cbc0000000p-1},
{-0x1.3cf9e20000000p-1,-0x1.9215100000000p-1},{-0x1.40e8800000000p-1,-0x1.8ef3780000000p-1},{-0x1.44cf320000000p-1,-0x1.8bc8060000000p-1},{-0x1.48ade20000000p-1,-0x1.8892d20000000p-1},
{-0x1.4c84740000000p-1,-0x1.8553ee0000000p-1},{-0x1.5052d40000000p-1,-0x1.820b700000000p-1},{-0x1.5418e60000000p-1,-0x1.7eb96a0000000p-1},{-0x1.57d6940000000p-1,-0x1.7b5df20000000p-1},
{-0x1.5b8bc60000000p-1,-0x1.77f91e0000000p-1},{-0x1.5f38640000000p-1,-0x1.748b040000000p-1},{-0x1.62dc580000000p-1,-0x1.7113b80000000p-1},{-0x1.66778c0000000p-1,-0x1.6d93520000000p-1},
{-0x1.6a09e60000000p-1,-0x1.6a09e60000000p-1},{-0x1.6d93520000000p-1,-0x1.66778c0000000p-1},{-0x1.7113b80000000p-1,-0x1.62dc580000000p-1},{-0x1.748b040000000p-1,-0x1.5f38640000000p-1},
{-0x1.77f91e0000000p-1,-0x1.5b8bc60000000p-1},{-0x1.7b5df20000000p-1,-0x1.57d6940000000p-1},{-0x1.7eb96a0000000p-1,-0x1.5418e60000000p-1},{-0x1.820b700000000p-1,-0x1.5052d40000000p-1},
{-0x1.8553ee0000000p-1,-0x1.4c84740000000p-1},{-0x1.8892d20000000p-1,-0x1.48ade20000000p-1},{-0x1.8bc8060000000p-1,-0x1.44cf320000000p-1},{-0x1.8ef3780000000p-1,-0x1.40e8800000000p-1},
{-0x1.9215100000000p-1,-0x1.3cf9e20000000p-1},{-0x1.952cbc0000000p-1,-0x1.3903720000000p-1},{-0x1.983a6a0000000p-1,-0x1.3505480000000p-1},{-0x1.9b3e040000000p-1,-0x1.30ff800000000p-1},
{-0x1.9e377a0000000p-1,-0x1.2cf2300000000p-1},{-0x1.a126b60000000p-1,-0x1.28dd740000000p-1},{-0x1.a40ba80000000p-1,-0x1.24c1640000000p-1},{-0x1.a6e63e0000000p-1,-0x1.209e1c0000000p-1},
{-0x1.a9b6620000000p-1,-0x1.1c73b40000000p-1},{-0x1.ac7c060000000p-1,-0x1.1842480000000p-1},{-0x1.af37180000000p-1,-0x1.1409f00000000p-1},{-0x1.b1e7880000000p-1,-0x1.0fcaca0000000p-1},
{-0x1.b48d400000000p-1,-0x1.0b84ee0000000p-1},{-0x1.b728340000000p-1,-0x1.07387a0000000p-1},{-0x1.b9b8520000000p-1,-0x1.02e5860000000p-1},{-0x1.bc3d8a0000000p-1,-0x1.fd185e0000000p-2},
{-0x1.beb7cc0000000p-1,-0x1.f459200000000p-2},{-0x1.c127080000000p-1,-0x1.eb8d8a0000000p-2},{-0x1.c38b300000000p-1,-0x1.e2b5d40000000p-2},{-0x1.c5e4320000000p-1,-0x1.d9d2340000000p-2},
{-0x1.c832020000000p-1,-0x1.d0e2e20000000p-2},{-0x1.ca74900000000p-1,-0x1.c7e81a0000000p-2},{-0x1.ccabce0000000p-1,-0x1.bee2100000000p-2},{-0x1.ced7b00000000p-1,-0x1.b5d1000000000p-2},
{-0x1.d0f8240000000p-1,-0x1.acb5240000000p-2},{-0x1.d30d200000000p-1,-0x1.a38eb20000000p-2},{-0x1.d516960000000p-1,-0x1.9a5de60000000p-2},{-0x1.d7147a0000000p-1,-0x1.9122fc0000000p-2},
{-0x1.d906bc0000000p-1,-0x1.87de2a0000000p-2},{-0x1.daed540000000p-1,-0x1.7e8fae0000000p-2},{-0x1.dcc8340000000p-1,-0x1.7537c20000000p-2},{-0x1.de97500000000p-1,-0x1.6bd69e0000000p-2},
{-0x1.e05a9e0000000p-1,-0x1.626c820000000p-2},{-0x1.e212100000000p-1,-0x1.58f9a80000000p-2},{-0x1.e3bd9e0000000p-1,-0x1.4f7e4a0000000p-2},{-0x1.e55d3c0000000p-1,-0x1.45faa40000000p-2},
{-0x1.e6f0e20000000p-1,-0x1.3c6ef40000000p-2},{-0x1.e878820000000p-1,-0x1.32db740000000p-2},{-0x1.e9f4160000000p-1,-0x1.2940620000000p-2},{-0x1.eb63920000000p-1,-0x1.1f9dfc0000000p-2},
{-0x1.ecc6ee0000000p-1,-0x1.15f47c0000000p-2},{-0x1.ee1e220000000p-1,-0x1.0c44200000000p-2},{-0x1.ef69260000000p-1,-0x1.028d260000000p-2},{-0x1.f0a7f00000000p-1,-0x1.f19f980000000p-3},
{-0x1.f1da780000000p-1,-0x1.de189a0000000p-3},{-0x1.f300b80000000p-1,-0x1.ca85d20000000p-3},{-0x1.f41aa80000000p-1,-0x1.b6e7b80000000p-3},{-0x1.f528420000000p-1,-0x1.a33eca0000000p-3},
{-0x1.f6297c0000000p-1,-0x1.8f8b840000000p-3},{-0x1.f71e540000000p-1,-0x1.7bce620000000p-3},{-0x1.f806c20000000p-1,-0x1.6807e20000000p-3},{-0x1.f8e2c00000000p-1,-0x1.54387e0000000p-3},
{-0x1.f9b24a0000000p-1,-0x1.4060b60000000p-3},{-0x1.fa75580000000p-1,-0x1.2c81060000000p-3},{-0x1.fb2be80000000p-1,-0x1.1899ee0000000p-3},{-0x1.fbd5f40000000p-1,-0x1.04abe80000000p-3},
{-0x1.fc73780000000p-1,-0x1.e16ee40000000p-4},{-0x1.fd04700000000p-1,-0x1.b97a1a0000000p-4},{-0x1.fd88da0000000p-1,-0x1.917a6c0000000p-4},{-0x1.fe00b20000000p-1,-0x1.6970d40000000p-4},
{-0x1.fe6bf20000000p-1,-0x1.415e540000000p-4},{-0x1.feca9c0000000p-1,-0x1.1943e40000000p-4},{-0x1.ff1cac0000000p-1,-0x1.e245060000000p-5},{-0x1.ff621e0000000p-1,-0x1.91f6600000000p-5},
{-0x1.ff9af20000000p-1,-0x1.419dce0000000p-5},{-0x1.ffc7280000000p-1,-0x1.e27a980000000p-6},{-0x1.ffe6bc0000000p-1,-0x1.41adac0000000p-6},{-0x1.fff9ae0000000p-1,-0x1.41b1a40000000p-7},
{-0x1.0000000000000p+0,-0x1.1a62640000000p-53},{-0x1.fff9ae0000000p-1,0x1.41b1a40000000p-7},{-0x1.ffe6bc0000000p-1,0x1.41adac0000000p-6},{-0x1.ffc7280000000p-1,0x1.e27a980000000p-6},
{-0x1.ff9af20000000p-1,0x1.419dce0000000p-5},{-0x1.ff621e0000000p-1,0x1.91f6600000000p-5},{-0x1.ff1cac0000000p-1,0x1.e245060000000p-5},{-0x1.feca9c0000000p-1,0x1.1943e40000000p-4},
{-0x1.fe6bf20000000p-1,0x1.415e540000000p-4},{-0x1.fe00b20000000p-1,0x1.6970d40000000p-4},{-0x1.fd88da0000000p-1,0x1.917a6c0000000p-4},{-0x1.fd04700000000p-1,0x1.b97a1a0000000p-4},
{-0x1.fc73780000000p-1,0x1.e16ee40000000p-4},{-0x1.fbd5f40000000p-1,0x1.04abe80000000p-3},{-0x1.fb2be80000000p-1,0x1.1899ee0000000p-3},{-0x1.fa75580000000p-1,0x1.2c81060000000p-3},
{-0x1.f9b24a0000000p-1,0x1.4060b60000000p-3},{-0x1.f8e2c00000000p-1,0x1.54387e0000000p-3},{-0x1.f806c20000000p-1,0x1.6807e20000000p-3},{-0x1.f71e540000000p-1,0x1.7bce620000000p-3},
{-0x1.f6297c0000000p-1,0x1.8f8b840000000p-3},{-0x1.f528420000000p-1,0x1.a33eca0000000p-3},{-0x1.f41aa80000000p-1,0x1.b6e7b80000000p-3},{-0x1.f300b80000000p-1,0x1.ca85d20000000p-3},
{-0x1.f1da780000000p-1,0x1.de189a0000000p-3},{-0x1.f0a7f00000000p-1,0x1.f19f980000000p-3},{-0x1.ef69260000000p-1,0x1.028d260000000p-2},{-0x1.ee1e220000000p-1,0x1.0c44200000000p-2},
{-0x1.ecc6ee0000000p-1,0x1.15f47c0000000p-2},{-0x1.eb63920000000p-1,0x1.1f9dfc0000000p-2},{-0x1.e9f4160000000p-1,0x1.2940620000000p-2},{-0x1.e878820000000p-1,0x1.32db740000000p-2},
{-0x1.e6f0e20000000p-1,0x1.3c6ef40000000p-2},{-0x1.e55d3c0000000p-1,0x1.45faa40000000p-2},{-0x1.e3bd9e0000000p-1,0x1.4f7e4a0000000p-2},{-0x1.e212100000000p-1,0x1.58f9a80000000p-2},
{-0x1.e05a9e0000000p-1,0x1.626c820000000p-2},{-0x1.de97500000000p-1,0x1.6bd69e0000000p-2},{-0x1.dcc8340000000p-1,0x1.7537c20000000p-2},{-0x1.daed540000000p-1,0x1.7e8fae0000000p-2},
{-0x1.d906bc0000000p-1,0x1.87de2a0000000p-2},{-0x1.d7147a0000000p-1,0x1.9122fc0000000p-2},{-0x1.d516960000000p-1,0x1.9a5de60000000p-2},{-0x1.d30d200000000p-1,0x1.a38eb20000000p-2},
{-0x1.d0f8240000000p-1,0x1.acb5240000000p-2},{-0x1.ced7b00000000p-1,0x1.b5d1000000000p-2},{-0x1.ccabce0000000p-1,0x1.bee2100000000p-2},{-0x1.ca74900000000p-1,0x1.c7e81a0000000p-2},
{-0x1.c832020000000p-1,0x1.d0e2e20000000p-2},{-0x1.c5e4320000000p-1,0x1.d9d2340000000p-2},{-0x1.c38b300000000p-1,0x1.e2b5d40000000p-2},{-0x1.c127080000000p-1,0x1.eb8d8a0000000p-2},
{-0x1.beb7cc0000000p-1,0x1.f459200000000p-2},{-0x1.bc3d8a0000000p-1,0x1.fd185e0000000p-2},{-0x1.b9b8520000000p-1,0x1.02e5860000000p-1},{-0x1.b728340000000p-1,0x1.07387a0000000p-1},
{-0x1.b48d400000000p-1,0x1.0b84ee0000000p-1},{-0x1.b1e7880000000p-1,0x1.0fcaca0000000p-1},{-0x1.af37180000000p-1,0x1.1409f00000000p-1},{-0x1.ac7c060000000p-1,0x1.1842480000000p-1},
{-0x1.a9b6620000000p-1,0x1.1c73b40000000p-1},{-0x1.a6e63e0000000p-1,0x1.209e1c0000000p-1},{-0x1.a40ba80000000p-1,0x1.24c1640000000p-1},{-0x1.a126b60000000p-1,0x1.28dd740000000p-1},
{-0x1.9e377a0000000p-1,0x1.2cf2300000000p-1},{-0x1.9b3e040000000p-1,0x1.30ff800000000p-1},{-0x1.983a6a0000000p-1,0x1.3505480000000p-1},{-0x1.952cbc0000000p-1,0x1.3903720000000p-1},
{-0x1.9215100000000p-1,0x1.3cf9e20000000p-1},{-0x1.8ef3780000000p-1,0x1.40e8800000000p-1},{-0x1.8bc8060000000p-1,0x1.44cf320000000p-1},{-0x1.8892d20000000p-1,0x1.48ade20000000p-1},
{-0x1.8553ee0000000p-1,0x1.4c84740000000p-1},{-0x1.820b700000000p-1,0x1.5052d40000000p-1},{-0x1.7eb96a0000000p-1,0x1.5418e60000000p-1},{-0x1.7b5df20000000p-1,0x1.57d6940000000p-1},
{-0x1.77f91e0000000p-1,0x1.5b8bc60000000p-1},{-0x1.748b040000000p-1,0x1.5f38640000000p-1},{-0x1.7113b80000000p-1,0x1.62dc580000000p-1},{-0x1.6d93520000000p-1,0x1.66778c0000000p-1},
{-0x1.6a09e60000000p-1,0x1.6a09e60000000p-1},{-0x1.66778c0000000p-1,0x1.6d93520000000p-1},{-0x1.62dc580000000p-1,0x1.7113b80000000p-1},{-0x1.5f38640000000p-1,0x1.748b040000000p-1},
{-0x1.5b8bc60000000p-1,0x1.77f91e0000000p-1},{-0x1.57d6940000000p-1,0x1.7b5df20000000p-1},{-0x1.5418e60000000p-1,0x1.7eb96a0000000p-1},{-0x1.5052d40000000p-1,0x1.820b700000000p-1},
{-0x1.4c84740000000p-1,0x1.8553ee0000000p-1},{-0x1.48ade20000000p-1,0x1.8892d20000000p-1},{-0x1.44cf320000000p-1,0x1.8bc8060000000p-1},{-0x1.40e8800000000p-1,0x1.8ef3780000000p-1},
{-0x1.3cf9e20000000p-1,0x1.9215100000000p-1},{-0x1.3903720000000p-1,0x1.952cbc0000000p-1},{-0x1.3505480000000p-1,0x1.983a6a0000000p-1},{-0x1.30ff800000000p-1,0x1.9b3e040000000p-1},
{-0x1.2cf2300000000p-1,0x1.9e377a0000000p-1},{-0x1.28dd740000000p-1,0x1.a126b60000000p-1},{-0x1.24c1640000000p-1,0x1.a40ba80000000p-1},{-0x1.209e1c0000000p-1,0x1.a6e63e0000000p-1},
{-0x1.1c73b40000000p-1,0x1.a9b6620000000p-1},{-0x1.1842480000000p-1,0x1.ac7c060000000p-1},{-0x1.1409f00000000p-1,0x1.af37180000000p-1},{-0x1.0fcaca0000000p-1,0x1.b1e7880000000p-1},
{-0x1.0b84ee0000000p-1,0x1.b48d400000000p-1},{-0x1.07387a0000000p-1,0x1.b728340000000p-1},{-0x1.02e5860000000p-1,0x1.b9b8520000000p-1},{-0x1.fd185e0000000p-2,0x1.bc3d8a0000000p-1},
{-0x1.f459200000000p-2,0x1.beb7cc0000000p-1},{-0x1.eb8d8a0000000p-2,0x1.c127080000000p-1},{-0x1.e2b5d40000000p-2,0x1.c38b300000000p-1},{-0x1.d9d2340000000p-2,0x1.c5e4320000000p-1},
{-0x1.d0e2e20000000p-2,0x1.c832020000000p-1},{-0x1.c7e81a0000000p-2,0x1.ca74900000000p-1},{-0x1.bee2100000000p-2,0x1.ccabce0000000p-1},{-0x1.b5d1000000000p-2,0x1.ced7b00000000p-1},
{-0x1.acb5240000000p-2,0x1.d0f8240000000p-1},{-0x1.a38eb20000000p-2,0x1.d30d200000000p-1},{-0x1.9a5de60000000p-2,0x1.d516960000000p-1},{-0x1.9122fc0000000p-2,0x1.d7147a0000000p-1},
{-0x1.87de2a0000000p-2,0x1.d906bc0000000p-1},{-0x1.7e8fae0000000p-2,0x1.daed540000000p-1},{-0x1.7537c20000000p-2,0x1.dcc8340000000p-1},{-0x1.6bd69e0000000p-2,0x1.de97500000000p-1},
{-0x1.626c820000000p-2,0x1.e05a9e0000000p-1},{-0x1.58f9a80000000p-2,0x1.e212100000000p-1},{-0x1.4f7e4a0000000p-2,0x1.e3bd9e0000000p-1},{-0x1.45faa40000000p-2,0x1.e55d3c0000000p-1},
{-0x1.3c6ef40000000p-2,0x1.e6f0e20000000p-1},{-0x1.32db740000000p-2,0x1.e878820000000p-1},{-0x1.2940620000000p-2,0x1.e9f4160000000p-1},{-0x1.1f9dfc0000000p-2,0x1.eb63920000000p-1},
{-0x1.15f47c0000000p-2,0x1.ecc6ee0000000p-1},{-0x1.0c44200000000p-2,0x1.ee1e220000000p-1},{-0x1.028d260000000p-2,0x1.ef69260000000p-1},{-0x1.f19f980000000p-3,0x1.f0a7f00000000p-1},
{-0x1.de189a0000000p-3,0x1.f1da780000000p-1},{-0x1.ca85d20000000p-3,0x1.f300b80000000p-1},{-0x1.b6e7b80000000p-3,0x1.f41aa80000000p-1},{-0x1.a33eca0000000p-3,0x1.f528420000000p-1},
{-0x1.8f8b840000000p-3,0x1.f6297c0000000p-1},{-0x1.7bce620000000p-3,0x1.f71e540000000p-1},{-0x1.6807e20000000p-3,0x1.f806c20000000p-1},{-0x1.54387e0000000p-3,0x1.f8e2c00000000p-1},
{-0x1.4060b60000000p-3,0x1.f9b24a0000000p-1},{-0x1.2c81060000000p-3,0x1.fa75580000000p-1},{-0x1.1899ee0000000p-3,0x1.fb2be80000000p-1},{-0x1.04abe80000000p-3,0x1.fbd5f40000000p-1},
{-0x1.e16ee40000000p-4,0x1.fc73780000000p-1},{-0x1.b97a1a0000000p-4,0x1.fd04700000000p-1},{-0x1.917a6c0000000p-4,0x1.fd88da0000000p-1},{-0x1.6970d40000000p-4,0x1.fe00b20000000p-1},
{-0x1.415e540000000p-4,0x1.fe6bf20000000p-1},{-0x1.1943e40000000p-4,0x1.feca9c0000000p-1},{-0x1.e245060000000p-5,0x1.ff1cac0000000p-1},{-0x1.91f6600000000p-5,0x1.ff621e0000000p-1},
{-0x1.419dce0000000p-5,0x1.ff9af20000000p-1},{-0x1.e27a980000000p-6,0x1.ffc7280000000p-1},{-0x1.41adac0000000p-6,0x1.ffe6bc0000000p-1},{-0x1.41b1a40000000p-7,0x1.fff9ae0000000p-1},
{-0x1.a793940000000p-53,0x1.0000000000000p+0},{0x1.41b1a40000000p-7,0x1.fff9ae0000000p-1},{0x1.41adac0000000p-6,0x1.ffe6bc0000000p-1},{0x1.e27a980000000p-6,0x1.ffc7280000000p-1},
{0x1.419dce0000000p-5,0x1.ff9af20000000p-1},{0x1.91f6600000000p-5,0x1.ff621e0000000p-1},{0x1.e245060000000p-5,0x1.ff1cac0000000p-1},{0x1.1943e40000000p-4,0x1.feca9c0000000p-1},
{0x1.415e540000000p-4,0x1.fe6bf20000000p-1},{0x1.6970d40000000p-4,0x1.fe00b20000000p-1},{0x1.917a6c0000000p-4,0x1.fd88da0000000p-1},{0x1.b97a1a0000000p-4,0x1.fd04700000000p-1},
{0x1.e16ee40000000p-4,0x1.fc73780000000p-1},{0x1.04abe80000000p-3,0x1.fbd5f40000000p-1},{0x1.1899ee0000000p-3,0x1.fb2be80000000p-1},{0x1.2c81060000000p-3,0x1.fa75580000000p-1},
{0x1.4060b60000000p-3,0x1.f9b24a0000000p-1},{0x1.54387e0000000p-3,0x1.f8e2c00000000p-1},{0x1.6807e20000000p-3,0x1.f806c20000000p-1},{0x1.7bce620000000p-3,0x1.f71e540000000p-1},
{0x1.8f8b840000000p-3,0x1.f6297c0000000p-1},{0x1.a33eca0000000p-3,0x1.f528420000000p-1},{0x1.b6e7b80000000p-3,0x1.f41aa80000000p-1},{0x1.ca85d20000000p-3,0x1.f300b80000000p-1},
{0x1.de189a0000000p-3,0x1.f1da780000000p-1},{0x1.f19f980000000p-3,0x1.f0a7f00000000p-1},{0x1.028d260000000p-2,0x1.ef69260000000p-1},{0x1.0c44200000000p-2,0x1.ee1e220000000p-1},
{0x1.15f47c0000000p-2,0x1.ecc6ee0000000p-1},{0x1.1f9dfc0000000p-2,0x1.eb63920000000p-1},{0x1.2940620000000p-2,0x1.e9f4160000000p-1},{0x1.32db740000000p-2,0x1.e878820000000p-1},
{0x1.3c6ef40000000p-2,0x1.e6f0e20000000p-1},{0x1.45faa40000000p-2,0x1.e55d3c0000000p-1},{0x1.4f7e4a0000000p-2,0x1.e3bd9e0000000p-1},{0x1.58f9a80000000p-2,0x1.e212100000000p-1},
{0x1.626c820000000p-2,0x1.e05a9e0000000p-1},{0x1.6bd69e0000000p-2,0x1.de97500000000p-1},{0x1.7537c20000000p-2,0x1.dcc8340000000p-1},{0x1.7e8fae0000000p-2,0x1.daed540000000p-1},
{0x1.87de2a0000000p-2,0x1.d906bc0000000p-1},{0x1.9122fc0000000p-2,0x1.d7147a0000000p-1},{0x1.9a5de60000000p-2,0x1.d516960000000p-1},{0x1.a38eb20000000p-2,0x1.d30d200000000p-1},
{0x1.acb5240000000p-2,0x1.d0f8240000000p-1},{0x1.b5d1000000000p-2,0x1.ced7b00000000p-1},{0x1.bee2100000000p-2,0x1.ccabce0000000p-1},{0x1.c7e81a0000000p-2,0x1.ca74900000000p-1},
{0x1.d0e2e20000000p-2,0x1.c832020000000p-1},{0x1.d9d2340000000p-2,0x1.c5e4320000000p-1},{0x1.e2b5d40000000p-2,0x1.c38b300000000p-1},{0x1.eb8d8a0000000p-2,0x1.c127080000000p-1},
{0x1.f459200000000p-2,0x1.beb7cc0000000p-1},{0x1.fd185e0000000p-2,0x1.bc3d8a0000000p-1},{0x1.02e5860000000p-1,0x1.b9b8520000000p-1},{0x1.07387a0000000p-1,0x1.b728340000000p-1},
{0x1.0b84ee0000000p-1,0x1.b48d400000000p-1},{0x1.0fcaca0000000p-1,0x1.b1e7880000000p-1},{0x1.1409f00000000p-1,0x1.af37180000000p-1},{0x1.1842480000000p-1,0x1.ac7c060000000p-1},
{0x1.1c73b40000000p-1,0x1.a9b6620000000p-1},{0x1.209e1c0000000p-1,0x1.a6e63e0000000p-1},{0x1.24c1640000000p-1,0x1.a40ba80000000p-1},{0x1.28dd740000000p-1,0x1.a126b60000000p-1},
{0x1.2cf2300000000p-1,0x1.9e377a0000000p-1},{0x1.30ff800000000p-1,0x1.9b3e040000000p-1},{0x1.3505480000000p-1,0x1.983a6a0000000p-1},{0x1.3903720000000p-1,0x1.952cbc0000000p-1},
{0x1.3cf9e20000000p-1,0x1.9215100000000p-1},{0x1.40e8800000000p-1,0x1.8ef3780000000p-1},{0x1.44cf320000000p-1,0x1.8bc8060000000p-1},{0x1.48ade20000000p-1,0x1.8892d20000000p-1},
{0x1.4c84740000000p-1,0x1.8553ee0000000p-1},{0x1.5052d40000000p-1,0x1.820b700000000p-1},{0x1.5418e60000000p-1,0x1.7eb96a0000000p-1},{0x1.57d6940000000p-1,0x1.7b5df20000000p-1},
{0x1.5b8bc60000000p-1,0x1.77f91e0000000p-1},{0x1.5f38640000000p-1,0x1.748b040000000p-1},{0x1.62dc580000000p-1,0x1.7113b80000000p-1},{0x1.66778c0000000p-1,0x1.6d93520000000p-1},
{0x1.6a09e60000000p-1,0x1.6a09e60000000p-1},{0x1.6d93520000000p-1,0x1.66778c0000000p-1},{0x1.7113b80000000p-1,0x1.62dc580000000p-1},{0x1.748b040000000p-1,0x1.5f38640000000p-1},
{0x1.77f91e0000000p-1,0x1.5b8bc60000000p-1},{0x1.7b5df20000000p-1,0x1.57d6940000000p-1},{0x1.7eb96a0000000p-1,0x1.5418e60000000p-1},{0x1.820b700000000p-1,0x1.5052d40000000p-1},
{0x1.8553ee0000000p-1,0x1.4c84740000000p-1},{0x1.8892d20000000p-1,0x1.48ade20000000p-1},{0x1.8bc8060000000p-1,0x1.44cf320000000p-1},{0x1.8ef3780000000p-1,0x1.40e8800000000p-1},
{0x1.9215100000000p-1,0x1.3cf9e20000000p-1},{0x1.952cbc0000000p-1,0x1.3903720000000p-1},{0x1.983a6a0000000p-1,0x1.3505480000000p-1},{0x1.9b3e040000000p-1,0x1.30ff800000000p-1},
{0x1.9e377a0000000p-1,0x1.2cf2300000000p-1},{0x1.a126b60000000p-1,0x1.28dd740000000p-1},{0x1.a40ba80000000p-1,0x1.24c1640000000p-1},{0x1.a6e63e0000000p-1,0x1.209e1c0000000p-1},
{0x1.a9b6620000000p-1,0x1.1c73b40000000p-1},{0x1.ac7c060000000p-1,0x1.1842480000000p-1},{0x1.af37180000000p-1,0x1.1409f00000000p-1},{0x1.b1e7880000000p-1,0x1.0fcaca0000000p-1},
{0x1.b48d400000000p-1,0x1.0b84ee0000000p-1},{0x1.b728340000000p-1,0x1.07387a0000000p-1},{0x1.b9b8520000000p-1,0x1.02e5860000000p-1},{0x1.bc3d8a0000000p-1,0x1.fd185e0000000p-2},
{0x1.beb7cc0000000p-1,0x1.f459200000000p-2},{0x1.c127080000000p-1,0x1.eb8d8a0000000p-2},{0x1.c38b300000000p-1,0x1.e2b5d40000000p-2},{0x1.c5e4320000000p-1,0x1.d9d2340000000p-2},
{0x1.c832020000000p-1,0x1.d0e2e20000000p-2},{0x1.ca74900000000p-1,0x1.c7e81a0000000p-2},{0x1.ccabce0000000p-1,0x1.bee2100000000p-2},{0x1.ced7b00000000p-1,0x1.b5d1000000000p-2},
{0x1.d0f8240000000p-1,0x1.acb5240000000p-2},{0x1.d30d200000000p-1,0x1.a38eb20000000p-2},{0x1.d516960000000p-1,0x1.9a5de60000000p-2},{0x1.d7147a0000000p-1,0x1.9122fc0000000p-2},
{0x1.d906bc0000000p-1,0x1.87de2a0000000p-2},{0x1.daed540000000p-1,0x1.7e8fae0000000p-2},{0x1.dcc8340000000p-1,0x1.7537c20000000p-2},{0x1.de97500000000p-1,0x1.6bd69e0000000p-2},
{0x1.e05a9e0000000p-1,0x1.626c820000000p-2},{0x1.e212100000000p-1,0x1.58f9a80000000p-2},{0x1.e3bd9e0000000p-1,0x1.4f7e4a0000000p-2},{0x1.e55d3c0000000p-1,0x1.45faa40000000p-2},
{0x1.e6f0e20000000p-1,0x1.3c6ef40000000p-2},{0x1.e878820000000p-1,0x1.32db740000000p-2},{0x1.e9f4160000000p-1,0x1.2940620000000p-2},{0x1.eb63920000000p-1,0x1.1f9dfc0000000p-2},
{0x1.ecc6ee0000000p-1,0x1.15f47c0000000p-2},{0x1.ee1e220000000p-1,0x1.0c44200000000p-2},{0x1.ef69260000000p-1,0x1.028d260000000p-2},{0x1.f0a7f00000000p-1,0x1.f19f980000000p-3},
{0x1.f1da780000000p-1,0x1.de189a0000000p-3},{0x1.f300b80000000p-1,0x1.ca85d20000000p-3},{0x1.f41aa80000000p-1,0x1.b6e7b80000000p-3},{0x1.f528420000000p-1,0x1.a33eca0000000p-3},
{0x1.f6297c0000000p-1,0x1.8f8b840000000p-3},{0x1.f71e540000000p-1,0x1.7bce620000000p-3},{0x1.f806c20000000p-1,0x1.6807e20000000p-3},{0x1.f8e2c00000000p-1,0x1.54387e0000000p-3},
{0x1.f9b24a0000000p-1,0x1.4060b60000000p-3},{0x1.fa75580000000p-1,0x1.2c81060000000p-3},{0x1.fb2be80000000p-1,0x1.1899ee0000000p-3},{0x1.fbd5f40000000p-1,0x1.04abe80000000p-3},
{0x1.fc73780000000p-1,0x1.e16ee40000000p-4},{0x1.fd04700000000p-1,0x1.b97a1a0000000p-4},{0x1.fd88da0000000p-1,0x1.917a6c0000000p-4},{0x1.fe00b20000000p-1,0x1.6970d40000000p-4},
{0x1.fe6bf20000000p-1,0x1.415e540000000p-4},{0x1.feca9c0000000p-1,0x1.1943e40000000p-4},{0x1.ff1cac0000000p-1,0x1.e245060000000p-5},{0x1.ff621e0000000p-1,0x1.91f6600000000p-5},
{0x1.ff9af20000000p-1,0x1.419dce0000000p-5},{0x1.ffc7280000000p-1,0x1.e27a980000000p-6},{0x1.ffe6bc0000000p-1,0x1.41adac0000000p-6},{0x1.fff9ae0000000p-1,0x1.41b1a40000000p-7},
};
__device__ const float2 TW2048[2048] = {
{0x1.0000000000000p+0,-0x0.0p+0},{0x1.ffff620000000p-1,-0x1.921f8c0000000p-9},{0x1.fffd880000000p-1,-0x1.921f100000000p-8},{0x1.fffa720000000p-1,-0x1.2d96b00000000p-7},
{0x1.fff6220000000p-1,-0x1.921d200000000p-7},{0x1.fff0940000000p-1,-0x1.f6a2960000000p-7},{0x1.ffe9cc0000000p-1,-0x1.2d936c0000000p-6},{0x1.ffe1c60000000p-1,-0x1.5fd4d20000000p-6},
{0x1.ffd8860000000p-1,-0x1.9215600000000p-6},{0x1.ffce0a0000000p-1,-0x1.c454f40000000p-6},{0x1.ffc2520000000p-1,-0x1.f693740000000p-6},{0x1.ffb55e0000000p-1,-0x1.14685e0000000p-5},
{0x1.ffa72e0000000p-1,-0x1.2d86580000000p-5},{0x1.ff97c40000000p-1,-0x1.46a3960000000p-5},{0x1.ff871e0000000p-1,-0x1.5fc00e0000000p-5},{0x1.ff753c0000000p-1,-0x1.78dbaa0000000p-5},
{0x1.ff621e0000000p-1,-0x1.91f6600000000p-5},{0x1.ff4dc60000000p-1,-0x1.ab101c0000000p-5},{0x1.ff38300000000p-1,-0x1.c428d20000000p-5},{0x1.ff21620000000p-1,-0x1.dd40700000000p-5},
{0x1.ff09560000000p-1,-0x1.f656e80000000p-5},{0x1.fef0100000000p-1,-0x1.07b6140000000p-4},{0x1.fed58e0000000p-1,-0x1.1440140000000p-4},{0x1.feb9d20000000p-1,-0x1.20c9680000000p-4},
{0x1.fe9cda0000000p-1,-0x1.2d520a0000000p-4},{0x1.fe7ea80000000p-1,-0x1.39d9f20000000p-4},{0x1.fe5f3a0000000p-1,-0x1.4661180000000p-4},{0x1.fe3e920000000p-1,-0x1.52e7740000000p-4},
{0x1.fe1cb00000000p-1,-0x1.5f6d000000000p-4},{0x1.fdf9920000000p-1,-0x1.6bf1b40000000p-4},{0x1.fdd53a0000000p-1,-0x1.7875860000000p-4},{0x1.fdafa80000000p-1,-0x1.84f8720000000p-4},
{0x1.fd88da0000000p-1,-0x1.917a6c0000000p-4},{0x1.fd60d20000000p-1,-0x1.9dfb6e0000000p-4},{0x1.fd37920000000p-1,-0x1.aa7b720000000p-4},{0x1.fd0d160000000p-1,-0x1.b6fa6e0000000p-4},
{0x1.fce1600000000p-1,-0x1.c3785c0000000p-4},{0x1.fcb4700000000p-1,-0x1.cff5340000000p-4},{0x1.fc86460000000p-1,-0x1.dc70ec0000000p-4},{0x1.fc56e40000000p-1,-0x1.e8eb800000000p-4},
{0x1.fc26480000000p-1,-0x1.f564e60000000p-4},{0x1.fbf4700000000p-1,-0x1.00ee8a0000000p-3},{0x1.fbc1620000000p-1,-0x1.072a040000000p-3},{0x1.fb8d180000000p-1,-0x1.0d64dc0000000p-3},
{0x1.fb57980000000p-1,-0x1.139f0c0000000p-3},{0x1.fb20dc0000000p-1,-0x1.19d8940000000p-3},{0x1.fae8e80000000p-1,-0x1.20116e0000000p-3},{0x1.faafbc0000000p-1,-0x1.2649940000000p-3},
{0x1.fa75580000000p-1,-0x1.2c81060000000p-3},{0x1.fa39ba0000000p-1,-0x1.32b7c00000000p-3},{0x1.f9fce60000000p-1,-0x1.38edbc0000000p-3},{0x1.f9bed80000000p-1,-0x1.3f22f60000000p-3},
{0x1.f97f920000000p-1,-0x1.45576c0000000p-3},{0x1.f93f140000000p-1,-0x1.4b8b180000000p-3},{0x1.f8fd600000000p-1,-0x1.51bdf80000000p-3},{0x1.f8ba740000000p-1,-0x1.57f0080000000p-3},
{0x1.f876500000000p-1,-0x1.5e21440000000p-3},{0x1.f830f40000000p-1,-0x1.6451a80000000p-3},{0x1.f7ea620000000p-1,-0x1.6a81300000000p-3},{0x1.f7a29a0000000p-1,-0x1.70afd80000000p-3},
{0x1.f7599a0000000p-1,-0x1.76dd9e0000000p-3},{0x1.f70f640000000p-1,-0x1.7d0a7c0000000p-3},{0x1.f6c3f80000000p-1,-0x1.83366e0000000p-3},{0x1.f677560000000p-1,-0x1.8961720000000p-3},
{0x1.f6297c0000000p-1,-0x1.8f8b840000000p-3},{0x1.f5da6e0000000p-1,-0x1.95b49e0000000p-3},{0x1.f58a2c0000000p-1,-0x1.9bdcc00000000p-3},{0x1.f538b20000000p-1,-0x1.a203e20000000p-3},
{0x1.f4e6040000000p-1,-0x1.a82a020000000p-3},{0x1.f492200000000p-1,-0x1.ae4f1e0000000p-3},{0x1.f43d080000000p-1,-0x1.b4732e0000000p-3},{0x1.f3e6bc0000000p-1,-0x1.ba96340000000p-3},
{0x1.f38f3a0000000p-1,-0x1.c0b8260000000p-3},{0x1.f336860000000p-1,-0x1.c6d9060000000p-3},{0x1.f2dc9c0000000p-1,-0x1.ccf8cc0000000p-3},{0x1.f281800000000p-1,-0x1.d317740000000p-3},
{0x1.f225300000000p-1,-0x1.d934fe0000000p-3},{0x1.f1c7ac0000000p-1,-0x1.df51640000000p-3},{0x1.f168f60000000p-1,-0x1.e56ca20000000p-3},{0x1.f1090c0000000p-1,-0x1.eb86b40000000p-3},
{0x1.f0a7f00000000p-1,-0x1.f19f980000000p-3},{0x1.f045a20000000p-1,-0x1.f7b7480000000p-3},{0x1.efe2200000000p-1,-0x1.fdcdc20000000p-3},{0x1.ef7d6e0000000p-1,-0x1.01f1800000000p-2},
{0x1.ef178a0000000p-1,-0x1.04fb800000000p-2},{0x1.eeb0740000000p-1,-0x1.0804e00000000p-2},{0x1.ee482e0000000p-1,-0x1.0b0d9c0000000p-2},{0x1.eddeb60000000p-1,-0x1.0e15b40000000p-2},
{0x1.ed740e0000000p-1,-0x1.111d260000000p-2},{0x1.ed08360000000p-1,-0x1.1423ee0000000p-2},{0x1.ec9b2e0000000p-1,-0x1.172a0e0000000p-2},{0x1.ec2cf40000000p-1,-0x1.1a2f800000000p-2},
{0x1.ebbd8c0000000p-1,-0x1.1d34440000000p-2},{0x1.eb4cf60000000p-1,-0x1.2038580000000p-2},{0x1.eadb2e0000000p-1,-0x1.233bba0000000p-2},{0x1.ea683a0000000p-1,-0x1.263e6a0000000p-2},
{0x1.e9f4160000000p-1,-0x1.2940620000000p-2},{0x1.e97ec40000000p-1,-0x1.2c41a40000000p-2},{0x1.e908440000000p-1,-0x1.2f422e0000000p-2},{0x1.e890960000000p-1,-0x1.3241fc0000000p-2},
{0x1.e817ba0000000p-1,-0x1.35410c0000000p-2},{0x1.e79db20000000p-1,-0x1.383f5e0000000p-2},{0x1.e7227e0000000p-1,-0x1.3b3cf00000000p-2},{0x1.e6a61c0000000p-1,-0x1.3e39be0000000p-2},
{0x1.e6288e0000000p-1,-0x1.4135ca0000000p-2},{0x1.e5a9d60000000p-1,-0x1.44310e0000000p-2},{0x1.e529f00000000p-1,-0x1.472b8a0000000p-2},{0x1.e4a8e00000000p-1,-0x1.4a253e0000000p-2},
{0x1.e426a40000000p-1,-0x1.4d1e240000000p-2},{0x1.e3a33e0000000p-1,-0x1.50163e0000000p-2},{0x1.e31eae0000000p-1,-0x1.530d880000000p-2},{0x1.e298f40000000p-1,-0x1.5604020000000p-2},
{0x1.e212100000000p-1,-0x1.58f9a80000000p-2},{0x1.e18a020000000p-1,-0x1.5bee780000000p-2},{0x1.e100cc0000000p-1,-0x1.5ee2740000000p-2},{0x1.e0766e0000000p-1,-0x1.61d5960000000p-2},
{0x1.dfeae60000000p-1,-0x1.64c7de0000000p-2},{0x1.df5e360000000p-1,-0x1.67b94a0000000p-2},{0x1.ded0600000000p-1,-0x1.6aa9d80000000p-2},{0x1.de41600000000p-1,-0x1.6d99860000000p-2},
{0x1.ddb13c0000000p-1,-0x1.7088540000000p-2},{0x1.dd1ff00000000p-1,-0x1.73763c0000000p-2},{0x1.dc8d7c0000000p-1,-0x1.7663400000000p-2},{0x1.dbf9e40000000p-1,-0x1.794f5e0000000p-2},
{0x1.db65260000000p-1,-0x1.7c3a940000000p-2},{0x1.dacf420000000p-1,-0x1.7f24de0000000p-2},{0x1.da383a0000000p-1,-0x1.820e3c0000000p-2},{0x1.d9a00e0000000p-1,-0x1.84f6aa0000000p-2},
{0x1.d906bc0000000p-1,-0x1.87de2a0000000p-2},{0x1.d86c480000000p-1,-0x1.8ac4b80000000p-2},{0x1.d7d0b00000000p-1,-0x1.8daa520000000p-2},{0x1.d733f60000000p-1,-0x1.908ef80000000p-2},
{0x1.d696180000000p-1,-0x1.9372a60000000p-2},{0x1.d5f7180000000p-1,-0x1.96555c0000000p-2},{0x1.d556f60000000p-1,-0x1.9937160000000p-2},{0x1.d4b5b20000000p-1,-0x1.9c17d40000000p-2},
{0x1.d4134e0000000p-1,-0x1.9ef7940000000p-2},{0x1.d36fc80000000p-1,-0x1.a1d6540000000p-2},{0x1.d2cb220000000p-1,-0x1.a4b4120000000p-2},{0x1.d2255c0000000p-1,-0x1.a790ce0000000p-2},
{0x1.d17e780000000p-1,-0x1.aa6c820000000p-2},{0x1.d0d6720000000p-1,-0x1.ad47320000000p-2},{0x1.d02d500000000p-1,-0x1.b020d60000000p-2},{0x1.cf830e0000000p-1,-0x1.b2f9720000000p-2},
{0x1.ced7b00000000p-1,-0x1.b5d1000000000p-2},{0x1.ce2b320000000p-1,-0x1.b8a7820000000p-2},{0x1.cd7d980000000p-1,-0x1.bb7cf20000000p-2},{0x1.cccee20000000p-1,-0x1.be51520000000p-2},
{0x1.cc1f100000000p-1,-0x1.c1249e0000000p-2},{0x1.cb6e200000000p-1,-0x1.c3f6d40000000p-2},{0x1.cabc160000000p-1,-0x1.c6c7f40000000p-2},{0x1.ca08f20000000p-1,-0x1.c997fc0000000p-2},
{0x1.c954b20000000p-1,-0x1.cc66ea0000000p-2},{0x1.c89f580000000p-1,-0x1.cf34ba0000000p-2},{0x1.c7e8e60000000p-1,-0x1.d2016e0000000p-2},{0x1.c731580000000p-1,-0x1.d4cd020000000p-2},
{0x1.c678b40000000p-1,-0x1.d797760000000p-2},{0x1.c5bef60000000p-1,-0x1.da60c60000000p-2},{0x1.c504200000000p-1,-0x1.dd28f20000000p-2},{0x1.c448340000000p-1,-0x1.dfeff60000000p-2},
{0x1.c38b300000000p-1,-0x1.e2b5d40000000p-2},{0x1.c2cd140000000p-1,-0x1.e57a860000000p-2},{0x1.c20de40000000p-1,-0x1.e83e0e0000000p-2},{0x1.c14d9e0000000p-1,-0x1.eb006a0000000p-2},
{0x1.c08c420000000p-1,-0x1.edc1960000000p-2},{0x1.bfc9d20000000p-1,-0x1.f081900000000p-2},{0x1.bf064e0000000p-1,-0x1.f3405a0000000p-2},{0x1.be41b60000000p-1,-0x1.f5fdee0000000p-2},
{0x1.bd7c0a0000000p-1,-0x1.f8ba4e0000000p-2},{0x1.bcb54c0000000p-1,-0x1.fb75760000000p-2},{0x1.bbed7c0000000p-1,-0x1.fe2f640000000p-2},{0x1.bb249a0000000p-1,-0x1.00740c0000000p-1},
{0x1.ba5aa60000000p-1,-0x1.01cfc80000000p-1},{0x1.b98fa20000000p-1,-0x1.032ae60000000p-1},{0x1.b8c38e0000000p-1,-0x1.0485620000000p-1},{0x1.b7f6680000000p-1,-0x1.05df3e0000000p-1},
{0x1.b728340000000p-1,-0x1.07387a0000000p-1},{0x1.b658f20000000p-1,-0x1.0891120000000p-1},{0x1.b588a00000000p-1,-0x1.09e9080000000p-1},{0x1.b4b7400000000p-1,-0x1.0b40580000000p-1},
{0x1.b3e4d40000000p-1,-0x1.0c97040000000p-1},{0x1.b3115a0000000p-1,-0x1.0ded0c0000000p-1},{0x1.b23cd40000000p-1,-0x1.0f426c0000000p-1},{0x1.b167420000000p-1,-0x1.1097240000000p-1},
{0x1.b090a60000000p-1,-0x1.11eb360000000p-1},{0x1.afb8fe0000000p-1,-0x1.133e9c0000000p-1},{0x1.aee04c0000000p-1,-0x1.14915a0000000p-1},{0x1.ae06900000000p-1,-0x1.15e36e0000000p-1},
{0x1.ad2bca0000000p-1,-0x1.1734d60000000p-1},{0x1.ac4ffc0000000p-1,-0x1.1885920000000p-1},{0x1.ab73260000000p-1,-0x1.19d5a00000000p-1},{0x1.aa95480000000p-1,-0x1.1b25020000000p-1},
{0x1.a9b6620000000p-1,-0x1.1c73b40000000p-1},{0x1.a8d6760000000p-1,-0x1.1dc1b60000000p-1},{0x1.a7f5860000000p-1,-0x1.1f0f080000000p-1},{0x1.a7138e0000000p-1,-0x1.205baa0000000p-1},
{0x1.a630920000000p-1,-0x1.21a79a0000000p-1},{0x1.a54c920000000p-1,-0x1.22f2d60000000p-1},{0x1.a4678c0000000p-1,-0x1.243d600000000p-1},{0x1.a381840000000p-1,-0x1.2587340000000p-1},
{0x1.a29a7a0000000p-1,-0x1.26d0540000000p-1},{0x1.a1b26e0000000p-1,-0x1.2818be0000000p-1},{0x1.a0c95e0000000p-1,-0x1.2960720000000p-1},{0x1.9fdf500000000p-1,-0x1.2aa76e0000000p-1},
{0x1.9ef43e0000000p-1,-0x1.2bedb20000000p-1},{0x1.9e082e0000000p-1,-0x1.2d333e0000000p-1},{0x1.9d1b200000000p-1,-0x1.2e780e0000000p-1},{0x1.9c2d120000000p-1,-0x1.2fbc240000000p-1},
{0x1.9b3e040000000p-1,-0x1.30ff800000000p-1},{0x1.9a4dfa0000000p-1,-0x1.32421e0000000p-1},{0x1.995cf20000000p-1,-0x1.3384000000000p-1},{0x1.986af00000000p-1,-0x1.34c5260000000p-1},
{0x1.9777f00000000p-1,-0x1.36058c0000000p-1},{0x1.9683f40000000p-1,-0x1.3745320000000p-1},{0x1.958efe0000000p-1,-0x1.3884180000000p-1},{0x1.94990e0000000p-1,-0x1.39c23e0000000p-1},
{0x1.93a2240000000p-1,-0x1.3affa20000000p-1},{0x1.92aa420000000p-1,-0x1.3c3c440000000p-1},{0x1.91b1660000000p-1,-0x1.3d78240000000p-1},{0x1.90b7940000000p-1,-0x1.3eb33e0000000p-1},
{0x1.8fbcca0000000p-1,-0x1.3fed960000000p-1},{0x1.8ec10a0000000p-1,-0x1.4127260000000p-1},{0x1.8dc4540000000p-1,-0x1.425ff20000000p-1},{0x1.8cc6a80000000p-1,-0x1.4397f60000000p-1},
{0x1.8bc8060000000p-1,-0x1.44cf320000000p-1},{0x1.8ac8720000000p-1,-0x1.4605a60000000p-1},{0x1.89c7ea0000000p-1,-0x1.473b520000000p-1},{0x1.88c66e0000000p-1,-0x1.4870340000000p-1},
{0x1.87c4000000000p-1,-0x1.49a44a0000000p-1},{0x1.86c0a20000000p-1,-0x1.4ad7960000000p-1},{0x1.85bc520000000p-1,-0x1.4c0a140000000p-1},{0x1.84b7120000000p-1,-0x1.4d3bc60000000p-1},
{0x1.83b0e00000000p-1,-0x1.4e6cac0000000p-1},{0x1.82a9c20000000p-1,-0x1.4f9cc20000000p-1},{0x1.81a1b40000000p-1,-0x1.50cc0a0000000p-1},{0x1.8098b80000000p-1,-0x1.51fa820000000p-1},
{0x1.7f8ece0000000p-1,-0x1.53282a0000000p-1},{0x1.7e83f80000000p-1,-0x1.5455000000000p-1},{0x1.7d78360000000p-1,-0x1.5581040000000p-1},{0x1.7c6b8a0000000p-1,-0x1.56ac360000000p-1},
{0x1.7b5df20000000p-1,-0x1.57d6940000000p-1},{0x1.7a4f700000000p-1,-0x1.59001e0000000p-1},{0x1.7940060000000p-1,-0x1.5a28d20000000p-1},{0x1.782fb20000000p-1,-0x1.5b50b20000000p-1},
{0x1.771e760000000p-1,-0x1.5c77bc0000000p-1},{0x1.760c520000000p-1,-0x1.5d9dee0000000p-1},{0x1.74f9480000000p-1,-0x1.5ec34a0000000p-1},{0x1.73e5580000000p-1,-0x1.5fe7cc0000000p-1},
{0x1.72d0840000000p-1,-0x1.610b760000000p-1},{0x1.71baca0000000p-1,-0x1.622e440000000p-1},{0x1.70a42c0000000p-1,-0x1.63503a0000000p-1},{0x1.6f8caa0000000p-1,-0x1.6471540000000p-1},
{0x1.6e74460000000p-1,-0x1.6591920000000p-1},{0x1.6d5afe0000000p-1,-0x1.66b0f40000000p-1},{0x1.6c40d80000000p-1,-0x1.67cf780000000p-1},{0x1.6b25ce0000000p-1,-0x1.68ed1e0000000p-1},
{0x1.6a09e60000000p-1,-0x1.6a09e60000000p-1},{0x1.68ed1e0000000p-1,-0x1.6b25ce0000000p-1},{0x1.67cf780000000p-1,-0x1.6c40d80000000p-1},{0x1.66b0f40000000p-1,-0x1.6d5afe0000000p-1},
{0x1.6591920000000p-1,-0x1.6e74460000000p-1},{0x1.6471540000000p-1,-0x1.6f8caa0000000p-1},{0x1.63503a0000000p-1,-0x1.70a42c0000000p-1},{0x1.622e440000000p-1,-0x1.71baca0000000p-1},
{0x1.610b760000000p-1,-0x1.72d0840000000p-1},{0x1.5fe7cc0000000p-1,-0x1.73e5580000000p-1},{0x1.5ec34a0000000p-1,-0x1.74f9480000000p-1},{0x1.5d9dee0000000p-1,-0x1.760c520000000p-1},
{0x1.5c77bc0000000p-1,-0x1.771e760000000p-1},{0x1.5b50b20000000p-1,-0x1.782fb20000000p-1},{0x1.5a28d20000000p-1,-0x1.7940060000000p-1},{0x1.59001e0000000p-1,-0x1.7a4f700000000p-1},
{0x1.57d6940000000p-1,-0x1.7b5df20000000p-1},{0x1.56ac360000000p-1,-0x1.7c6b8a0000000p-1},{0x1.5581040000000p-1,-0x1.7d78360000000p-1},{0x1.5455000000000p-1,-0x1.7e83f80000000p-1},
{0x1.53282a0000000p-1,-0x1.7f8ece0000000p-1},{0x1.51fa820000000p-1,-0x1.8098b80000000p-1},{0x1.50cc0a0000000p-1,-0x1.81a1b40000000p-1},{0x1.4f9cc20000000p-1,-0x1.82a9c20000000p-1},
{0x1.4e6cac0000000p-1,-0x1.83b0e00000000p-1},{0x1.4d3bc60000000p-1,-0x1.84b7120000000p-1},{0x1.4c0a140000000p-1,-0x1.85bc520000000p-1},{0x1.4ad7960000000p-1,-0x1.86c0a20000000p-1},
{0x1.49a44a0000000p-1,-0x1.87c4000000000p-1},{0x1.4870340000000p-1,-0x1.88c66e0000000p-1},{0x1.473b520000000p-1,-0x1.89c7ea0000000p-1},{0x1.4605a60000000p-1,-0x1.8ac8720000000p-1},
{0x1.44cf320000000p-1,-0x1.8bc8060000000p-1},{0x1.4397f60000000p-1,-0x1.8cc6a80000000p-1},{0x1.425ff20000000p-1,-0x1.8dc4540000000p-1},{0x1.4127260000000p-1,-0x1.8ec10a0000000p-1},
{0x1.3fed960000000p-1,-0x1.8fbcca0000000p-1},{0x1.3eb33e0000000p-1,-0x1.90b7940000000p-1},{0x1.3d78240000000p-1,-0x1.91b1660000000p-1},{0x1.3c3c440000000p-1,-0x1.92aa420000000p-1},
{0x1.3affa20000000p-1,-0x1.93a2240000000p-1},{0x1.39c23e0000000p-1,-0x1.94990e0000000p-1},{0x1.3884180000000p-1,-0x1.958efe0000000p-1},{0x1.3745320000000p-1,-0x1.9683f40000000p-1},
{0x1.36058c0000000p-1,-0x1.9777f00000000p-1},{0x1.34c5260000000p-1,-0x1.986af00000000p-1},{0x1.3384000000000p-1,-0x1.995cf20000000p-1},{0x1.32421e0000000p-1,-0x1.9a4dfa0000000p-1},
{0x1.30ff800000000p-1,-0x1.9b3e040000000p-1},{0x1.2fbc240000000p-1,-0x1.9c2d120000000p-1},{0x1.2e780e0000000p-1,-0x1.9d1b200000000p-1},{0x1.2d333e0000000p-1,-0x1.9e082e0000000p-1},
{0x1.2bedb20000000p-1,-0x1.9ef43e0000000p-1},{0x1.2aa76e0000000p-1,-0x1.9fdf500000000p-1},{0x1.2960720000000p-1,-0x1.a0c95e0000000p-1},{0x1.2818be0000000p-1,-0x1.a1b26e0000000p-1},
{0x1.26d0540000000p-1,-0x1.a29a7a0000000p-1},{0x1.2587340000000p-1,-0x1.a381840000000p-1},{0x1.243d600000000p-1,-0x1.a4678c0000000p-1},{0x1.22f2d60000000p-1,-0x1.a54c920000000p-1},
{0x1.21a79a0000000p-1,-0x1.a630920000000p-1},{0x1.205baa0000000p-1,-0x1.a7138e0000000p-1},{0x1.1f0f080000000p-1,-0x1.a7f5860000000p-1},{0x1.1dc1b60000000p-1,-0x1.a8d6760000000p-1},
{0x1.1c73b40000000p-1,-0x1.a9b6620000000p-1},{0x1.1b25020000000p-1,-0x1.aa95480000000p-1},{0x1.19d5a00000000p-1,-0x1.ab73260000000p-1},{0x1.1885920000000p-1,-0x1.ac4ffc0000000p-1},
{0x1.1734d60000000p-1,-0x1.ad2bca0000000p-1},{0x1.15e36e0000000p-1,-0x1.ae06900000000p-1},{0x1.14915a0000000p-1,-0x1.aee04c0000000p-1},{0x1.133e9c0000000p-1,-0x1.afb8fe0000000p-1},
{0x1.11eb360000000p-1,-0x1.b090a60000000p-1},{0x1.1097240000000p-1,-0x1.b167420000000p-1},{0x1.0f426c0000000p-1,-0x1.b23cd40000000p-1},{0x1.0ded0c0000000p-1,-0x1.b3115a0000000p-1},
{0x1.0c97040000000p-1,-0x1.b3e4d40000000p-1},{0x1.0b40580000000p-1,-0x1.b4b7400000000p-1},{0x1.09e9080000000p-1,-0x1.b588a00000000p-1},{0x1.0891120000000p-1,-0x1.b658f20000000p-1},
{0x1.07387a0000000p-1,-0x1.b728340000000p-1},{0x1.05df3e0000000p-1,-0x1.b7f6680000000p-1},{0x1.0485620000000p-1,-0x1.b8c38e0000000p-1},{0x1.032ae60000000p-1,-0x1.b98fa20000000p-1},
{0x1.01cfc80000000p-1,-0x1.ba5aa60000000p-1},{0x1.00740c0000000p-1,-0x1.bb249a0000000p-1},{0x1.fe2f640000000p-2,-0x1.bbed7c0000000p-1},{0x1.fb75760000000p-2,-0x1.bcb54c0000000p-1},
{0x1.f8ba4e0000000p-2,-0x1.bd7c0a0000000p-1},{0x1.f5fdee0000000p-2,-0x1.be41b60000000p-1},{0x1.f3405a0000000p-2,-0x1.bf064e0000000p-1},{0x1.f081900000000p-2,-0x1.bfc9d20000000p-1},
{0x1.edc1960000000p-2,-0x1.c08c420000000p-1},{0x1.eb006a0000000p-2,-0x1.c14d9e0000000p-1},{0x1.e83e0e0000000p-2,-0x1.c20de40000000p-1},{0x1.e57a860000000p-2,-0x1.c2cd140000000p-1},
{0x1.e2b5d40000000p-2,-0x1.c38b300000000p-1},{0x1.dfeff60000000p-2,-0x1.c448340000000p-1},{0x1.dd28f20000000p-2,-0x1.c504200000000p-1},{0x1.da60c60000000p-2,-0x1.c5bef60000000p-1},
{0x1.d797760000000p-2,-0x1.c678b40000000p-1},{0x1.d4cd020000000p-2,-0x1.c731580000000p-1},{0x1.d2016e0000000p-2,-0x1.c7e8e60000000p-1},{0x1.cf34ba0000000p-2,-0x1.c89f580000000p-1},
{0x1.cc66ea0000000p-2,-0x1.c954b20000000p-1},{0x1.c997fc0000000p-2,-0x1.ca08f20000000p-1},{0x1.c6c7f40000000p-2,-0x1.cabc160000000p-1},{0x1.c3f6d40000000p-2,-0x1.cb6e200000000p-1},
{0x1.c1249e0000000p-2,-0x1.cc1f100000000p-1},{0x1.be51520000000p-2,-0x1.cccee20000000p-1},{0x1.bb7cf20000000p-2,-0x1.cd7d980000000p-1},{0x1.b8a7820000000p-2,-0x1.ce2b320000000p-1},
{0x1.b5d1000000000p-2,-0x1.ced7b00000000p-1},{0x1.b2f9720000000p-2,-0x1.cf830e0000000p-1},{0x1.b020d60000000p-2,-0x1.d02d500000000p-1},{0x1.ad47320000000p-2,-0x1.d0d6720000000p-1},
{0x1.aa6c820000000p-2,-0x1.d17e780000000p-1},{0x1.a790ce0000000p-2,-0x1.d2255c0000000p-1},{0x1.a4b4120000000p-2,-0x1.d2cb220000000p-1},{0x1.a1d6540000000p-2,-0x1.d36fc80000000p-1},
{0x1.9ef7940000000p-2,-0x1.d4134e0000000p-1},{0x1.9c17d40000000p-2,-0x1.d4b5b20000000p-1},{0x1.9937160000000p-2,-0x1.d556f60000000p-1},{0x1.96555c0000000p-2,-0x1.d5f7180000000p-1},
{0x1.9372a60000000p-2,-0x1.d696180000000p-1},{0x1.908ef80000000p-2,-0x1.d733f60000000p-1},{0x1.8daa520000000p-2,-0x1.d7d0b00000000p-1},{0x1.8ac4b80000000p-2,-0x1.d86c480000000p-1},
{0x1.87de2a0000000p-2,-0x1.d906bc0000000p-1},{0x1.84f6aa0000000p-2,-0x1.d9a00e0000000p-1},{0x1.820e3c0000000p-2,-0x1.da383a0000000p-1},{0x1.7f24de0000000p-2,-0x1.dacf420000000p-1},
{0x1.7c3a940000000p-2,-0x1.db65260000000p-1},{0x1.794f5e0000000p-2,-0x1.dbf9e40000000p-1},{0x1.7663400000000p-2,-0x1.dc8d7c0000000p-1},{0x1.73763c0000000p-2,-0x1.dd1ff00000000p-1},
{0x1.7088540000000p-2,-0x1.ddb13c0000000p-1},{0x1.6d99860000000p-2,-0x1.de41600000000p-1},{0x1.6aa9d80000000p-2,-0x1.ded0600000000p-1},{0x1.67b94a0000000p-2,-0x1.df5e360000000p-1},
{0x1.64c7de0000000p-2,-0x1.dfeae60000000p-1},{0x1.61d5960000000p-2,-0x1.e0766e0000000p-1},{0x1.5ee2740000000p-2,-0x1.e100cc0000000p-1},{0x1.5bee780000000p-2,-0x1.e18a020000000p-1},
{0x1.58f9a80000000p-2,-0x1.e212100000000p-1},{0x1.5604020000000p-2,-0x1.e298f40000000p-1},{0x1.530d880000000p-2,-0x1.e31eae0000000p-1},{0x1.50163e0000000p-2,-0x1.e3a33e0000000p-1},
{0x1.4d1e240000000p-2,-0x1.e426a40000000p-1},{0x1.4a253e0000000p-2,-0x1.e4a8e00000000p-1},{0x1.472b8a0000000p-2,-0x1.e529f00000000p-1},{0x1.44310e0000000p-2,-0x1.e5a9d60000000p-1},
{0x1.4135ca0000000p-2,-0x1.e6288e0000000p-1},{0x1.3e39be0000000p-2,-0x1.e6a61c0000000p-1},{0x1.3b3cf00000000p-2,-0x1.e7227e0000000p-1},{0x1.383f5e0000000p-2,-0x1.e79db20000000p-1},
{0x1.35410c0000000p-2,-0x1.e817ba0000000p-1},{0x1.3241fc0000000p-2,-0x1.e890960000000p-1},{0x1.2f422e0000000p-2,-0x1.e908440000000p-1},{0x1.2c41a40000000p-2,-0x1.e97ec40000000p-1},
{0x1.2940620000000p-2,-0x1.e9f4160000000p-1},{0x1.263e6a0000000p-2,-0x1.ea683a0000000p-1},{0x1.233bba0000000p-2,-0x1.eadb2e0000000p-1},{0x1.2038580000000p-2,-0x1.eb4cf60000000p-1},
{0x1.1d34440000000p-2,-0x1.ebbd8c0000000p-1},{0x1.1a2f800000000p-2,-0x1.ec2cf40000000p-1},{0x1.172a0e0000000p-2,-0x1.ec9b2e0000000p-1},{0x1.1423ee0000000p-2,-0x1.ed08360000000p-1},
{0x1.111d260000000p-2,-0x1.ed740e0000000p-1},{0x1.0e15b40000000p-2,-0x1.eddeb60000000p-1},{0x1.0b0d9c0000000p-2,-0x1.ee482e0000000p-1},{0x1.0804e00000000p-2,-0x1.eeb0740000000p-1},
{0x1.04fb800000000p-2,-0x1.ef178a0000000p-1},{0x1.01f1800000000p-2,-0x1.ef7d6e0000000p-1},{0x1.fdcdc20000000p-3,-0x1.efe2200000000p-1},{0x1.f7b7480000000p-3,-0x1.f045a20000000p-1},
{0x1.f19f980000000p-3,-0x1.f0a7f00000000p-1},{0x1.eb86b40000000p-3,-0x1.f1090c0000000p-1},{0x1.e56ca20000000p-3,-0x1.f168f60000000p-1},{0x1.df51640000000p-3,-0x1.f1c7ac0000000p-1},
{0x1.d934fe0000000p-3,-0x1.f225300000000p-1},{0x1.d317740000000p-3,-0x1.f281800000000p-1},{0x1.ccf8cc0000000p-3,-0x1.f2dc9c0000000p-1},{0x1.c6d9060000000p-3,-0x1.f336860000000p-1},
{0x1.c0b8260000000p-3,-0x1.f38f3a0000000p-1},{0x1.ba96340000000p-3,-0x1.f3e6bc0000000p-1},{0x1.b4732e0000000p-3,-0x1.f43d080000000p-1},{0x1.ae4f1e0000000p-3,-0x1.f492200000000p-1},
{0x1.a82a020000000p-3,-0x1.f4e6040000000p-1},{0x1.a203e20000000p-3,-0x1.f538b20000000p-1},{0x1.9bdcc00000000p-3,-0x1.f58a2c0000000p-1},{0x1.95b49e0000000p-3,-0x1.f5da6e0000000p-1},
{0x1.8f8b840000000p-3,-0x1.f6297c0000000p-1},{0x1.8961720000000p-3,-0x1.f677560000000p-1},{0x1.83366e0000000p-3,-0x1.f6c3f80000000p-1},{0x1.7d0a7c0000000p-3,-0x1.f70f640000000p-1},
{0x1.76dd9e0000000p-3,-0x1.f7599a0000000p-1},{0x1.70afd80000000p-3,-0x1.f7a29a0000000p-1},{0x1.6a81300000000p-3,-0x1.f7ea620000000p-1},{0x1.6451a80000000p-3,-0x1.f830f40000000p-1},
{0x1.5e21440000000p-3,-0x1.f876500000000p-1},{0x1.57f0080000000p-3,-0x1.f8ba740000000p-1},{0x1.51bdf80000000p-3,-0x1.f8fd600000000p-1},{0x1.4b8b180000000p-3,-0x1.f93f140000000p-1},
{0x1.45576c0000000p-3,-0x1.f97f920000000p-1},{0x1.3f22f60000000p-3,-0x1.f9bed80000000p-1},{0x1.38edbc0000000p-3,-0x1.f9fce60000000p-1},{0x1.32b7c00000000p-3,-0x1.fa39ba0000000p-1},
{0x1.2c81060000000p-3,-0x1.fa75580000000p-1},{0x1.2649940000000p-3,-0x1.faafbc0000000p-1},{0x1.20116e0000000p-3,-0x1.fae8e80000000p-1},{0x1.19d8940000000p-3,-0x1.fb20dc0000000p-1},
{0x1.139f0c0000000p-3,-0x1.fb57980000000p-1},{0x1.0d64dc0000000p-3,-0x1.fb8d180000000p-1},{0x1.072a040000000p-3,-0x1.fbc1620000000p-1},{0x1.00ee8a0000000p-3,-0x1.fbf4700000000p-1},
{0x1.f564e60000000p-4,-0x1.fc26480000000p-1},{0x1.e8eb800000000p-4,-0x1.fc56e40000000p-1},{0x1.dc70ec0000000p-4,-0x1.fc86460000000p-1},{0x1.cff5340000000p-4,-0x1.fcb4700000000p-1},
{0x1.c3785c0000000p-4,-0x1.fce1600000000p-1},{0x1.b6fa6e0000000p-4,-0x1.fd0d160000000p-1},{0x1.aa7b720000000p-4,-0x1.fd37920000000p-1},{0x1.9dfb6e0000000p-4,-0x1.fd60d20000000p-1},
{0x1.917a6c0000000p-4,-0x1.fd88da0000000p-1},{0x1.84f8720000000p-4,-0x1.fdafa80000000p-1},{0x1.7875860000000p-4,-0x1.fdd53a0000000p-1},{0x1.6bf1b40000000p-4,-0x1.fdf9920000000p-1},
{0x1.5f6d000000000p-4,-0x1.fe1cb00000000p-1},{0x1.52e7740000000p-4,-0x1.fe3e920000000p-1},{0x1.4661180000000p-4,-0x1.fe5f3a0000000p-1},{0x1.39d9f20000000p-4,-0x1.fe7ea80000000p-1},
{0x1.2d520a0000000p-4,-0x1.fe9cda0000000p-1},{0x1.20c9680000000p-4,-0x1.feb9d20000000p-1},{0x1.1440140000000p-4,-0x1.fed58e0000000p-1},{0x1.07b6140000000p-4,-0x1.fef0100000000p-1},
{0x1.f656e80000000p-5,-0x1.ff09560000000p-1},{0x1.dd40700000000p-5,-0x1.ff21620000000p-1},{0x1.c428d20000000p-5,-0x1.ff38300000000p-1},{0x1.ab101c0000000p-5,-0x1.ff4dc60000000p-1},
{0x1.91f6600000000p-5,-0x1.ff621e0000000p-1},{0x1.78dbaa0000000p-5,-0x1.ff753c0000000p-1},{0x1.5fc00e0000000p-5,-0x1.ff871e0000000p-1},{0x1.46a3960000000p-5,-0x1.ff97c40000000p-1},
{0x1.2d86580000000p-5,-0x1.ffa72e0000000p-1},{0x1.14685e0000000p-5,-0x1.ffb55e0000000p-1},{0x1.f693740000000p-6,-0x1.ffc2520000000p-1},{0x1.c454f40000000p-6,-0x1.ffce0a0000000p-1},
{0x1.9215600000000p-6,-0x1.ffd8860000000p-1},{0x1.5fd4d20000000p-6,-0x1.ffe1c60000000p-1},{0x1.2d936c0000000p-6,-0x1.ffe9cc0000000p-1},{0x1.f6a2960000000p-7,-0x1.fff0940000000p-1},
{0x1.921d200000000p-7,-0x1.fff6220000000p-1},{0x1.2d96b00000000p-7,-0x1.fffa720000000p-1},{0x1.921f100000000p-8,-0x1.fffd880000000p-1},{0x1.921f8c0000000p-9,-0x1.ffff620000000p-1},
{0x1.1a62640000000p-54,-0x1.0000000000000p+0},{-0x1.921f8c0000000p-9,-0x1.ffff620000000p-1},{-0x1.921f100000000p-8,-0x1.fffd880000000p-1},{-0x1.2d96b00000000p-7,-0x1.fffa720000000p-1},
{-0x1.921d200000000p-7,-0x1.fff6220000000p-1},{-0x1.f6a2960000000p-7,-0x1.fff0940000000p-1},{-0x1.2d936c0000000p-6,-0x1.ffe9cc0000000p-1},{-0x1.5fd4d20000000p-6,-0x1.ffe1c60000000p-1},
{-0x1.9215600000000p-6,-0x1.ffd8860000000p-1},{-0x1.c454f40000000p-6,-0x1.ffce0a0000000p-1},{-0x1.f693740000000p-6,-0x1.ffc2520000000p-1},{-0x1.14685e0000000p-5,-0x1.ffb55e0000000p-1},
{-0x1.2d86580000000p-5,-0x1.ffa72e0000000p-1},{-0x1.46a3960000000p-5,-0x1.ff97c40000000p-1},{-0x1.5fc00e0000000p-5,-0x1.ff871e0000000p-1},{-0x1.78dbaa0000000p-5,-0x1.ff753c0000000p-1},
{-0x1.91f6600000000p-5,-0x1.ff621e0000000p-1},{-0x1.ab101c0000000p-5,-0x1.ff4dc60000000p-1},{-0x1.c428d20000000p-5,-0x1.ff38300000000p-1},{-0x1.dd40700000000p-5,-0x1.ff21620000000p-1},
{-0x1.f656e80000000p-5,-0x1.ff09560000000p-1},{-0x1.07b6140000000p-4,-0x1.fef0100000000p-1},{-0x1.1440140000000p-4,-0x1.fed58e0000000p-1},{-0x1.20c9680000000p-4,-0x1.feb9d20000000p-1},
{-0x1.2d520a0000000p-4,-0x1.fe9cda0000000p-1},{-0x1.39d9f20000000p-4,-0x1.fe7ea80000000p-1},{-0x1.4661180000000p-4,-0x1.fe5f3a0000000p-1},{-0x1.52e7740000000p-4,-0x1.fe3e920000000p-1},
{-0x1.5f6d000000000p-4,-0x1.fe1cb00000000p-1},{-0x1.6bf1b40000000p-4,-0x1.fdf9920000000p-1},{-0x1.7875860000000p-4,-0x1.fdd53a0000000p-1},{-0x1.84f8720000000p-4,-0x1.fdafa80000000p-1},
{-0x1.917a6c0000000p-4,-0x1.fd88da0000000p-1},{-0x1.9dfb6e0000000p-4,-0x1.fd60d20000000p-1},{-0x1.aa7b720000000p-4,-0x1.fd37920000000p-1},{-0x1.b6fa6e0000000p-4,-0x1.fd0d160000000p-1},
{-0x1.c3785c0000000p-4,-0x1.fce1600000000p-1},{-0x1.cff5340000000p-4,-0x1.fcb4700000000p-1},{-0x1.dc70ec0000000p-4,-0x1.fc86460000000p-1},{-0x1.e8eb800000000p-4,-0x1.fc56e40000000p-1},
{-0x1.f564e60000000p-4,-0x1.fc26480000000p-1},{-0x1.00ee8a0000000p-3,-0x1.fbf4700000000p-1},{-0x1.072a040000000p-3,-0x1.fbc1620000000p-1},{-0x1.0d64dc0000000p-3,-0x1.fb8d180000000p-1},
{-0x1.139f0c0000000p-3,-0x1.fb57980000000p-1},{-0x1.19d8940000000p-3,-0x1.fb20dc0000000p-1},{-0x1.20116e0000000p-3,-0x1.fae8e80000000p-1},{-0x1.2649940000000p-3,-0x1.faafbc0000000p-1},
{-0x1.2c81060000000p-3,-0x1.fa75580000000p-1},{-0x1.32b7c00000000p-3,-0x1.fa39ba0000000p-1},{-0x1.38edbc0000000p-3,-0x1.f9fce60000000p-1},{-0x1.3f22f60000000p-3,-0x1.f9bed80000000p-1},
{-0x1.45576c0000000p-3,-0x1.f97f920000000p-1},{-0x1.4b8b180000000p-3,-0x1.f93f140000000p-1},{-0x1.51bdf80000000p-3,-0x1.f8fd600000000p-1},{-0x1.57f0080000000p-3,-0x1.f8ba740000000p-1},
{-0x1.5e21440000000p-3,-0x1.f876500000000p-1},{-0x1.6451a80000000p-3,-0x1.f830f40000000p-1},{-0x1.6a81300000000p-3,-0x1.f7ea620000000p-1},{-0x1.70afd80000000p-3,-0x1.f7a29a0000000p-1},
{-0x1.76dd9e0000000p-3,-0x1.f7599a0000000p-1},{-0x1.7d0a7c0000000p-3,-0x1.f70f640000000p-1},{-0x1.83366e0000000p-3,-0x1.f6c3f80000000p-1},{-0x1.8961720000000p-3,-0x1.f677560000000p-1},
{-0x1.8f8b840000000p-3,-0x1.f6297c0000000p-1},{-0x1.95b49e0000000p-3,-0x1.f5da6e0000000p-1},{-0x1.9bdcc00000000p-3,-0x1.f58a2c0000000p-1},{-0x1.a203e20000000p-3,-0x1.f538b20000000p-1},
{-0x1.a82a020000000p-3,-0x1.f4e6040000000p-1},{-0x1.ae4f1e0000000p-3,-0x1.f492200000000p-1},{-0x1.b4732e0000000p-3,-0x1.f43d080000000p-1},{-0x1.ba96340000000p-3,-0x1.f3e6bc0000000p-1},
{-0x1.c0b8260000000p-3,-0x1.f38f3a0000000p-1},{-0x1.c6d9060000000p-3,-0x1.f336860000000p-1},{-0x1.ccf8cc0000000p-3,-0x1.f2dc9c0000000p-1},{-0x1.d317740000000p-3,-0x1.f281800000000p-1},
{-0x1.d934fe0000000p-3,-0x1.f225300000000p-1},{-0x1.df51640000000p-3,-0x1.f1c7ac0000000p-1},{-0x1.e56ca20000000p-3,-0x1.f168f60000000p-1},{-0x1.eb86b40000000p-3,-0x1.f1090c0000000p-1},
{-0x1.f19f980000000p-3,-0x1.f0a7f00000000p-1},{-0x1.f7b7480000000p-3,-0x1.f045a20000000p-1},{-0x1.fdcdc20000000p-3,-0x1.efe2200000000p-1},{-0x1.01f1800000000p-2,-0x1.ef7d6e0000000p-1},
{-0x1.04fb800000000p-2,-0x1.ef178a0000000p-1},{-0x1.0804e00000000p-2,-0x1.eeb0740000000p-1},{-0x1.0b0d9c0000000p-2,-0x1.ee482e0000000p-1},{-0x1.0e15b40000000p-2,-0x1.eddeb60000000p-1},
{-0x1.111d260000000p-2,-0x1.ed740e0000000p-1},{-0x1.1423ee0000000p-2,-0x1.ed08360000000p-1},{-0x1.172a0e0000000p-2,-0x1.ec9b2e0000000p-1},{-0x1.1a2f800000000p-2,-0x1.ec2cf40000000p-1},
{-0x1.1d34440000000p-2,-0x1.ebbd8c0000000p-1},{-0x1.2038580000000p-2,-0x1.eb4cf60000000p-1},{-0x1.233bba0000000p-2,-0x1.eadb2e0000000p-1},{-0x1.263e6a0000000p-2,-0x1.ea683a0000000p-1},
{-0x1.2940620000000p-2,-0x1.e9f4160000000p-1},{-0x1.2c41a40000000p-2,-0x1.e97ec40000000p-1},{-0x1.2f422e0000000p-2,-0x1.e908440000000p-1},{-0x1.3241fc0000000p-2,-0x1.e890960000000p-1},
{-0x1.35410c0000000p-2,-0x1.e817ba0000000p-1},{-0x1.383f5e0000000p-2,-0x1.e79db20000000p-1},{-0x1.3b3cf00000000p-2,-0x1.e7227e0000000p-1},{-0x1.3e39be0000000p-2,-0x1.e6a61c0000000p-1},
{-0x1.4135ca0000000p-2,-0x1.e6288e0000000p-1},{-0x1.44310e0000000p-2,-0x1.e5a9d60000000p-1},{-0x1.472b8a0000000p-2,-0x1.e529f00000000p-1},{-0x1.4a253e0000000p-2,-0x1.e4a8e00000000p-1},
{-0x1.4d1e240000000p-2,-0x1.e426a40000000p-1},{-0x1.50163e0000000p-2,-0x1.e3a33e0000000p-1},{-0x1.530d880000000p-2,-0x1.e31eae0000000p-1},{-0x1.5604020000000p-2,-0x1.e298f40000000p-1},
{-0x1.58f9a80000000p-2,-0x1.e212100000000p-1},{-0x1.5bee780000000p-2,-0x1.e18a020000000p-1},{-0x1.5ee2740000000p-2,-0x1.e100cc0000000p-1},{-0x1.61d5960000000p-2,-0x1.e0766e0000000p-1},
{-0x1.64c7de0000000p-2,-0x1.dfeae60000000p-1},{-0x1.67b94a0000000p-2,-0x1.df5e360000000p-1},{-0x1.6aa9d80000000p-2,-0x1.ded0600000000p-1},{-0x1.6d99860000000p-2,-0x1.de41600000000p-1},
{-0x1.7088540000000p-2,-0x1.ddb13c0000000p-1},{-0x1.73763c0000000p-2,-0x1.dd1ff00000000p-1},{-0x1.7663400000000p-2,-0x1.dc8d7c0000000p-1},{-0x1.794f5e0000000p-2,-0x1.dbf9e40000000p-1},
{-0x1.7c3a940000000p-2,-0x1.db65260000000p-1},{-0x1.7f24de0000000p-2,-0x1.dacf420000000p-1},{-0x1.820e3c0000000p-2,-0x1.da383a0000000p-1},{-0x1.84f6aa0000000p-2,-0x1.d9a00e0000000p-1},
{-0x1.87de2a0000000p-2,-0x1.d906bc0000000p-1},{-0x1.8ac4b80000000p-2,-0x1.d86c480000000p-1},{-0x1.8daa520000000p-2,-0x1.d7d0b00000000p-1},{-0x1.908ef80000000p-2,-0x1.d733f60000000p-1},
{-0x1.9372a60000000p-2,-0x1.d696180000000p-1},{-0x1.96555c0000000p-2,-0x1.d5f7180000000p-1},{-0x1.9937160000000p-2,-0x1.d556f60000000p-1},{-0x1.9c17d40000000p-2,-0x1.d4b5b20000000p-1},
{-0x1.9ef7940000000p-2,-0x1.d4134e0000000p-1},{-0x1.a1d6540000000p-2,-0x1.d36fc80000000p-1},{-0x1.a4b4120000000p-2,-0x1.d2cb220000000p-1},{-0x1.a790ce0000000p-2,-0x1.d2255c0000000p-1},
{-0x1.aa6c820000000p-2,-0x1.d17e780000000p-1},{-0x1.ad47320000000p-2,-0x1.d0d6720000000p-1},{-0x1.b020d60000000p-2,-0x1.d02d500000000p-1},{-0x1.b2f9720000000p-2,-0x1.cf830e0000000p-1},
{-0x1.b5d1000000000p-2,-0x1.ced7b00000000p-1},{-0x1.b8a7820000000p-2,-0x1.ce2b320000000p-1},{-0x1.bb7cf20000000p-2,-0x1.cd7d980000000p-1},{-0x1.be51520000000p-2,-0x1.cccee20000000p-1},
{-0x1.c1249e0000000p-2,-0x1.cc1f100000000p-1},{-0x1.c3f6d40000000p-2,-0x1.cb6e200000000p-1},{-0x1.c6c7f40000000p-2,-0x1.cabc160000000p-1},{-0x1.c997fc0000000p-2,-0x1.ca08f20000000p-1},
{-0x1.cc66ea0000000p-2,-0x1.c954b20000000p-1},{-0x1.cf34ba0000000p-2,-0x1.c89f580000000p-1},{-0x1.d2016e0000000p-2,-0x1.c7e8e60000000p-1},{-0x1.d4cd020000000p-2,-0x1.c731580000000p-1},
{-0x1.d797760000000p-2,-0x1.c678b40000000p-1},{-0x1.da60c60000000p-2,-0x1.c5bef60000000p-1},{-0x1.dd28f20000000p-2,-0x1.c504200000000p-1},{-0x1.dfeff60000000p-2,-0x1.c448340000000p-1},
{-0x1.e2b5d40000000p-2,-0x1.c38b300000000p-1},{-0x1.e57a860000000p-2,-0x1.c2cd140000000p-1},{-0x1.e83e0e0000000p-2,-0x1.c20de40000000p-1},{-0x1.eb006a0000000p-2,-0x1.c14d9e0000000p-1},
{-0x1.edc1960000000p-2,-0x1.c08c420000000p-1},{-0x1.f081900000000p-2,-0x1.bfc9d20000000p-1},{-0x1.f3405a0000000p-2,-0x1.bf064e0000000p-1},{-0x1.f5fdee0000000p-2,-0x1.be41b60000000p-1},
{-0x1.f8ba4e0000000p-2,-0x1.bd7c0a0000000p-1},{-0x1.fb75760000000p-2,-0x1.bcb54c0000000p-1},{-0x1.fe2f640000000p-2,-0x1.bbed7c0000000p-1},{-0x1.00740c0000000p-1,-0x1.bb249a0000000p-1},
{-0x1.01cfc80000000p-1,-0x1.ba5aa60000000p-1},{-0x1.032ae60000000p-1,-0x1.b98fa20000000p-1},{-0x1.0485620000000p-1,-0x1.b8c38e0000000p-1},{-0x1.05df3e0000000p-1,-0x1.b7f6680000000p-1},
{-0x1.07387a0000000p-1,-0x1.b728340000000p-1},{-0x1.0891120000000p-1,-0x1.b658f20000000p-1},{-0x1.09e9080000000p-1,-0x1.b588a00000000p-1},{-0x1.0b40580000000p-1,-0x1.b4b7400000000p-1},
{-0x1.0c97040000000p-1,-0x1.b3e4d40000000p-1},{-0x1.0ded0c0000000p-1,-0x1.b3115a0000000p-1},{-0x1.0f426c0000000p-1,-0x1.b23cd40000000p-1},{-0x1.1097240000000p-1,-0x1.b167420000000p-1},
{-0x1.11eb360000000p-1,-0x1.b090a60000000p-1},{-0x1.133e9c0000000p-1,-0x1.afb8fe0000000p-1},{-0x1.14915a0000000p-1,-0x1.aee04c0000000p-1},{-0x1.15e36e0000000p-1,-0x1.ae06900000000p-1},
{-0x1.1734d60000000p-1,-0x1.ad2bca0000000p-1},{-0x1.1885920000000p-1,-0x1.ac4ffc0000000p-1},{-0x1.19d5a00000000p-1,-0x1.ab73260000000p-1},{-0x1.1b25020000000p-1,-0x1.aa95480000000p-1},
{-0x1.1c73b40000000p-1,-0x1.a9b6620000000p-1},{-0x1.1dc1b60000000p-1,-0x1.a8d6760000000p-1},{-0x1.1f0f080000000p-1,-0x1.a7f5860000000p-1},{-0x1.205baa0000000p-1,-0x1.a7138e0000000p-1},
{-0x1.21a79a0000000p-1,-0x1.a630920000000p-1},{-0x1.22f2d60000000p-1,-0x1.a54c920000000p-1},{-0x1.243d600000000p-1,-0x1.a4678c0000000p-1},{-0x1.2587340000000p-1,-0x1.a381840000000p-1},
{-0x1.26d0540000000p-1,-0x1.a29a7a0000000p-1},{-0x1.2818be0000000p-1,-0x1.a1b26e0000000p-1},{-0x1.2960720000000p-1,-0x1.a0c95e0000000p-1},{-0x1.2aa76e0000000p-1,-0x1.9fdf500000000p-1},
{-0x1.2bedb20000000p-1,-0x1.9ef43e0000000p-1},{-0x1.2d333e0000000p-1,-0x1.9e082e0000000p-1},{-0x1.2e780e0000000p-1,-0x1.9d1b200000000p-1},{-0x1.2fbc240000000p-1,-0x1.9c2d120000000p-1},
{-0x1.30ff800000000p-1,-0x1.9b3e040000000p-1},{-0x1.32421e0000000p-1,-0x1.9a4dfa0000000p-1},{-0x1.3384000000000p-1,-0x1.995cf20000000p-1},{-0x1.34c5260000000p-1,-0x1.986af00000000p-1},
{-0x1.36058c0000000p-1,-0x1.9777f00000000p-1},{-0x1.3745320000000p-1,-0x1.9683f40000000p-1},{-0x1.3884180000000p-1,-0x1.958efe0000000p-1},{-0x1.39c23e0000000p-1,-0x1.94990e0000000p-1},
{-0x1.3affa20000000p-1,-0x1.93a2240000000p-1},{-0x1.3c3c440000000p-1,-0x1.92aa420000000p-1},{-0x1.3d78240000000p-1,-0x1.91b1660000000p-1},{-0x1.3eb33e0000000p-1,-0x1.90b7940000000p-1},
{-0x1.3fed960000000p-1,-0x1.8fbcca0000000p-1},{-0x1.4127260000000p-1,-0x1.8ec10a0000000p-1},{-0x1.425ff20000000p-1,-0x1.8dc4540000000p-1},{-0x1.4397f60000000p-1,-0x1.8cc6a80000000p-1},
{-0x1.44cf320000000p-1,-0x1.8bc8060000000p-1},{-0x1.4605a60000000p-1,-0x1.8ac8720000000p-1},{-0x1.473b520000000p-1,-0x1.89c7ea0000000p-1},{-0x1.4870340000000p-1,-0x1.88c66e0000000p-1},
{-0x1.49a44a0000000p-1,-0x1.87c4000000000p-1},{-0x1.4ad7960000000p-1,-0x1.86c0a20000000p-1},{-0x1.4c0a140000000p-1,-0x1.85bc520000000p-1},{-0x1.4d3bc60000000p-1,-0x1.84b7120000000p-1},
{-0x1.4e6cac0000000p-1,-0x1.83b0e00000000p-1},{-0x1.4f9cc20000000p-1,-0x1.82a9c20000000p-1},{-0x1.50cc0a0000000p-1,-0x1.81a1b40000000p-1},{-0x1.51fa820000000p-1,-0x1.8098b80000000p-1},
{-0x1.53282a0000000p-1,-0x1.7f8ece0000000p-1},{-0x1.5455000000000p-1,-0x1.7e83f80000000p-1},{-0x1.5581040000000p-1,-0x1.7d78360000000p-1},{-0x1.56ac360000000p-1,-0x1.7c6b8a0000000p-1},
{-0x1.57d6940000000p-1,-0x1.7b5df20000000p-1},{-0x1.59001e0000000p-1,-0x1.7a4f700000000p-1},{-0x1.5a28d20000000p-1,-0x1.7940060000000p-1},{-0x1.5b50b20000000p-1,-0x1.782fb20000000p-1},
{-0x1.5c77bc0000000p-1,-0x1.771e760000000p-1},{-0x1.5d9dee0000000p-1,-0x1.760c520000000p-1},{-0x1.5ec34a0000000p-1,-0x1.74f9480000000p-1},{-0x1.5fe7cc0000000p-1,-0x1.73e5580000000p-1},
{-0x1.610b760000000p-1,-0x1.72d0840000000p-1},{-0x1.622e440000000p-1,-0x1.71baca0000000p-1},{-0x1.63503a0000000p-1,-0x1.70a42c0000000p-1},{-0x1.6471540000000p-1,-0x1.6f8caa0000000p-1},
{-0x1.6591920000000p-1,-0x1.6e74460000000p-1},{-0x1.66b0f40000000p-1,-0x1.6d5afe0000000p-1},{-0x1.67cf780000000p-1,-0x1.6c40d80000000p-1},{-0x1.68ed1e0000000p-1,-0x1.6b25ce0000000p-1},
{-0x1.6a09e60000000p-1,-0x1.6a09e60000000p-1},{-0x1.6b25ce0000000p-1,-0x1.68ed1e0000000p-1},{-0x1.6c40d80000000p-1,-0x1.67cf780000000p-1},{-0x1.6d5afe0000000p-1,-0x1.66b0f40000000p-1},
{-0x1.6e74460000000p-1,-0x1.6591920000000p-1},{-0x1.6f8caa0000000p-1,-0x1.6471540000000p-1},{-0x1.70a42c0000000p-1,-0x1.63503a0000000p-1},{-0x1.71baca0000000p-1,-0x1.622e440000000p-1},
{-0x1.72d0840000000p-1,-0x1.610b760000000p-1},{-0x1.73e5580000000p-1,-0x1.5fe7cc0000000p-1},{-0x1.74f9480000000p-1,-0x1.5ec34a0000000p-1},{-0x1.760c520000000p-1,-0x1.5d9dee0000000p-1},
{-0x1.771e760000000p-1,-0x1.5c77bc0000000p-1},{-0x1.782fb20000000p-1,-0x1.5b50b20000000p-1},{-0x1.7940060000000p-1,-0x1.5a28d20000000p-1},{-0x1.7a4f700000000p-1,-0x1.59001e0000000p-1},
{-0x1.7b5df20000000p-1,-0x1.57d6940000000p-1},{-0x1.7c6b8a0000000p-1,-0x1.56ac360000000p-1},{-0x1.7d78360000000p-1,-0x1.5581040000000p-1},{-0x1.7e83f80000000p-1,-0x1.5455000000000p-1},
{-0x1.7f8ece0000000p-1,-0x1.53282a0000000p-1},{-0x1.8098b80000000p-1,-0x1.51fa820000000p-1},{-0x1.81a1b40000000p-1,-0x1.50cc0a0000000p-1},{-0x1.82a9c20000000p-1,-0x1.4f9cc20000000p-1},
{-0x1.83b0e00000000p-1,-0x1.4e6cac0000000p-1},{-0x1.84b7120000000p-1,-0x1.4d3bc60000000p-1},{-0x1.85bc520000000p-1,-0x1.4c0a140000000p-1},{-0x1.86c0a20000000p-1,-0x1.4ad7960000000p-1},
{-0x1.87c4000000000p-1,-0x1.49a44a0000000p-1},{-0x1.88c66e0000000p-1,-0x1.4870340000000p-1},{-0x1.89c7ea0000000p-1,-0x1.473b520000000p-1},{-0x1.8ac8720000000p-1,-0x1.4605a60000000p-1},
{-0x1.8bc8060000000p-1,-0x1.44cf320000000p-1},{-0x1.8cc6a80000000p-1,-0x1.4397f60000000p-1},{-0x1.8dc4540000000p-1,-0x1.425ff20000000p-1},{-0x1.8ec10a0000000p-1,-0x1.4127260000000p-1},
{-0x1.8fbcca0000000p-1,-0x1.3fed960000000p-1},{-0x1.90b7940000000p-1,-0x1.3eb33e0000000p-1},{-0x1.91b1660000000p-1,-0x1.3d78240000000p-1},{-0x1.92aa420000000p-1,-0x1.3c3c440000000p-1},
{-0x1.93a2240000000p-1,-0x1.3affa20000000p-1},{-0x1.94990e0000000p-1,-0x1.39c23e0000000p-1},{-0x1.958efe0000000p-1,-0x1.3884180000000p-1},{-0x1.9683f40000000p-1,-0x1.3745320000000p-1},
{-0x1.9777f00000000p-1,-0x1.36058c0000000p-1},{-0x1.986af00000000p-1,-0x1.34c5260000000p-1},{-0x1.995cf20000000p-1,-0x1.3384000000000p-1},{-0x1.9a4dfa0000000p-1,-0x1.32421e0000000p-1},
{-0x1.9b3e040000000p-1,-0x1.30ff800000000p-1},{-0x1.9c2d120000000p-1,-0x1.2fbc240000000p-1},{-0x1.9d1b200000000p-1,-0x1.2e780e0000000p-1},{-0x1.9e082e0000000p-1,-0x1.2d333e0000000p-1},
{-0x1.9ef43e0000000p-1,-0x1.2bedb20000000p-1},{-0x1.9fdf500000000p-1,-0x1.2aa76e0000000p-1},{-0x1.a0c95e0000000p-1,-0x1.2960720000000p-1},{-0x1.a1b26e0000000p-1,-0x1.2818be0000000p-1},
{-0x1.a29a7a0000000p-1,-0x1.26d0540000000p-1},{-0x1.a381840000000p-1,-0x1.2587340000000p-1},{-0x1.a4678c0000000p-1,-0x1.243d600000000p-1},{-0x1.a54c920000000p-1,-0x1.22f2d60000000p-1},
{-0x1.a630920000000p-1,-0x1.21a79a0000000p-1},{-0x1.a7138e0000000p-1,-0x1.205baa0000000p-1},{-0x1.a7f5860000000p-1,-0x1.1f0f080000000p-1},{-0x1.a8d6760000000p-1,-0x1.1dc1b60000000p-1},
{-0x1.a9b6620000000p-1,-0x1.1c73b40000000p-1},{-0x1.aa95480000000p-1,-0x1.1b25020000000p-1},{-0x1.ab73260000000p-1,-0x1.19d5a00000000p-1},{-0x1.ac4ffc0000000p-1,-0x1.1885920000000p-1},
{-0x1.ad2bca0000000p-1,-0x1.1734d60000000p-1},{-0x1.ae06900000000p-1,-0x1.15e36e0000000p-1},{-0x1.aee04c0000000p-1,-0x1.14915a0000000p-1},{-0x1.afb8fe0000000p-1,-0x1.133e9c0000000p-1},
{-0x1.b090a60000000p-1,-0x1.11eb360000000p-1},{-0x1.b167420000000p-1,-0x1.1097240000000p-1},{-0x1.b23cd40000000p-1,-0x1.0f426c0000000p-1},{-0x1.b3115a0000000p-1,-0x1.0ded0c0000000p-1},
{-0x1.b3e4d40000000p-1,-0x1.0c97040000000p-1},{-0x1.b4b7400000000p-1,-0x1.0b40580000000p-1},{-0x1.b588a00000000p-1,-0x1.09e9080000000p-1},{-0x1.b658f20000000p-1,-0x1.0891120000000p-1},
{-0x1.b728340000000p-1,-0x1.07387a0000000p-1},{-0x1.b7f6680000000p-1,-0x1.05df3e0000000p-1},{-0x1.b8c38e0000000p-1,-0x1.0485620000000p-1},{-0x1.b98fa20000000p-1,-0x1.032ae60000000p-1},
{-0x1.ba5aa60000000p-1,-0x1.01cfc80000000p-1},{-0x1.bb249a0000000p-1,-0x1.00740c0000000p-1},{-0x1.bbed7c0000000p-1,-0x1.fe2f640000000p-2},{-0x1.bcb54c0000000p-1,-0x1.fb75760000000p-2},
{-0x1.bd7c0a0000000p-1,-0x1.f8ba4e0000000p-2},{-0x1.be41b60000000p-1,-0x1.f5fdee0000000p-2},{-0x1.bf064e0000000p-1,-0x1.f3405a0000000p-2},{-0x1.bfc9d20000000p-1,-0x1.f081900000000p-2},
{-0x1.c08c420000000p-1,-0x1.edc1960000000p-2},{-0x1.c14d9e0000000p-1,-0x1.eb006a0000000p-2},{-0x1.c20de40000000p-1,-0x1.e83e0e0000000p-2},{-0x1.c2cd140000000p-1,-0x1.e57a860000000p-2},
{-0x1.c38b300000000p-1,-0x1.e2b5d40000000p-2},{-0x1.c448340000000p-1,-0x1.dfeff60000000p-2},{-0x1.c504200000000p-1,-0x1.dd28f20000000p-2},{-0x1.c5bef60000000p-1,-0x1.da60c60000000p-2},
{-0x1.c678b40000000p-1,-0x1.d797760000000p-2},{-0x1.c731580000000p-1,-0x1.d4cd020000000p-2},{-0x1.c7e8e60000000p-1,-0x1.d2016e0000000p-2},{-0x1.c89f580000000p-1,-0x1.cf34ba0000000p-2},
{-0x1.c954b20000000p-1,-0x1.cc66ea0000000p-2},{-0x1.ca08f20000000p-1,-0x1.c997fc0000000p-2},{-0x1.cabc160000000p-1,-0x1.c6c7f40000000p-2},{-0x1.cb6e200000000p-1,-0x1.c3f6d40000000p-2},
{-0x1.cc1f100000000p-1,-0x1.c1249e0000000p-2},{-0x1.cccee20000000p-1,-0x1.be51520000000p-2},{-0x1.cd7d980000000p-1,-0x1.bb7cf20000000p-2},{-0x1.ce2b320000000p-1,-0x1.b8a7820000000p-2},
{-0x1.ced7b00000000p-1,-0x1.b5d1000000000p-2},{-0x1.cf830e0000000p-1,-0x1.b2f9720000000p-2},{-0x1.d02d500000000p-1,-0x1.b020d60000000p-2},{-0x1.d0d6720000000p-1,-0x1.ad47320000000p-2},
{-0x1.d17e780000000p-1,-0x1.aa6c820000000p-2},{-0x1.d2255c0000000p-1,-0x1.a790ce0000000p-2},{-0x1.d2cb220000000p-1,-0x1.a4b4120000000p-2},{-0x1.d36fc80000000p-1,-0x1.a1d6540000000p-2},
{-0x1.d4134e0000000p-1,-0x1.9ef7940000000p-2},{-0x1.d4b5b20000000p-1,-0x1.9c17d40000000p-2},{-0x1.d556f60000000p-1,-0x1.9937160000000p-2},{-0x1.d5f7180000000p-1,-0x1.96555c0000000p-2},
{-0x1.d696180000000p-1,-0x1.9372a60000000p-2},{-0x1.d733f60000000p-1,-0x1.908ef80000000p-2},{-0x1.d7d0b00000000p-1,-0x1.8daa520000000p-2},{-0x1.d86c480000000p-1,-0x1.8ac4b80000000p-2},
{-0x1.d906bc0000000p-1,-0x1.87de2a0000000p-2},{-0x1.d9a00e0000000p-1,-0x1.84f6aa0000000p-2},{-0x1.da383a0000000p-1,-0x1.820e3c0000000p-2},{-0x1.dacf420000000p-1,-0x1.7f24de0000000p-2},
{-0x1.db65260000000p-1,-0x1.7c3a940000000p-2},{-0x1.dbf9e40000000p-1,-0x1.794f5e0000000p-2},{-0x1.dc8d7c0000000p-1,-0x1.7663400000000p-2},{-0x1.dd1ff00000000p-1,-0x1.73763c0000000p-2},
{-0x1.ddb13c0000000p-1,-0x1.7088540000000p-2},{-0x1.de41600000000p-1,-0x1.6d99860000000p-2},{-0x1.ded0600000000p-1,-0x1.6aa9d80000000p-2},{-0x1.df5e360000000p-1,-0x1.67b94a0000000p-2},
{-0x1.dfeae60000000p-1,-0x1.64c7de0000000p-2},{-0x1.e0766e0000000p-1,-0x1.61d5960000000p-2},{-0x1.e100cc0000000p-1,-0x1.5ee2740000000p-2},{-0x1.e18a020000000p-1,-0x1.5bee780000000p-2},
{-0x1.e212100000000p-1,-0x1.58f9a80000000p-2},{-0x1.e298f40000000p-1,-0x1.5604020000000p-2},{-0x1.e31eae0000000p-1,-0x1.530d880000000p-2},{-0x1.e3a33e0000000p-1,-0x1.50163e0000000p-2},
{-0x1.e426a40000000p-1,-0x1.4d1e240000000p-2},{-0x1.e4a8e00000000p-1,-0x1.4a253e0000000p-2},{-0x1.e529f00000000p-1,-0x1.472b8a0000000p-2},{-0x1.e5a9d60000000p-1,-0x1.44310e0000000p-2},
{-0x1.e6288e0000000p-1,-0x1.4135ca0000000p-2},{-0x1.e6a61c0000000p-1,-0x1.3e39be0000000p-2},{-0x1.e7227e0000000p-1,-0x1.3b3cf00000000p-2},{-0x1.e79db20000000p-1,-0x1.383f5e0000000p-2},
{-0x1.e817ba0000000p-1,-0x1.35410c0000000p-2},{-0x1.e890960000000p-1,-0x1.3241fc0000000p-2},{-0x1.e908440000000p-1,-0x1.2f422e0000000p-2},{-0x1.e97ec40000000p-1,-0x1.2c41a40000000p-2},
{-0x1.e9f4160000000p-1,-0x1.2940620000000p-2},{-0x1.ea683a0000000p-1,-0x1.263e6a0000000p-2},{-0x1.eadb2e0000000p-1,-0x1.233bba0000000p-2},{-0x1.eb4cf60000000p-1,-0x1.2038580000000p-2},
{-0x1.ebbd8c0000000p-1,-0x1.1d34440000000p-2},{-0x1.ec2cf40000000p-1,-0x1.1a2f800000000p-2},{-0x1.ec9b2e0000000p-1,-0x1.172a0e0000000p-2},{-0x1.ed08360000000p-1,-0x1.1423ee0000000p-2},
{-0x1.ed740e0000000p-1,-0x1.111d260000000p-2},{-0x1.eddeb60000000p-1,-0x1.0e15b40000000p-2},{-0x1.ee482e0000000p-1,-0x1.0b0d9c0000000p-2},{-0x1.eeb0740000000p-1,-0x1.0804e00000000p-2},
{-0x1.ef178a0000000p-1,-0x1.04fb800000000p-2},{-0x1.ef7d6e0000000p-1,-0x1.01f1800000000p-2},{-0x1.efe2200000000p-1,-0x1.fdcdc20000000p-3},{-0x1.f045a20000000p-1,-0x1.f7b7480000000p-3},
{-0x1.f0a7f00000000p-1,-0x1.f19f980000000p-3},{-0x1.f1090c0000000p-1,-0x1.eb86b40000000p-3},{-0x1.f168f60000000p-1,-0x1.e56ca20000000p-3},{-0x1.f1c7ac0000000p-1,-0x1.df51640000000p-3},
{-0x1.f225300000000p-1,-0x1.d934fe0000000p-3},{-0x1.f281800000000p-1,-0x1.d317740000000p-3},{-0x1.f2dc9c0000000p-1,-0x1.ccf8cc0000000p-3},{-0x1.f336860000000p-1,-0x1.c6d9060000000p-3},
{-0x1.f38f3a0000000p-1,-0x1.c0b8260000000p-3},{-0x1.f3e6bc0000000p-1,-0x1.ba96340000000p-3},{-0x1.f43d080000000p-1,-0x1.b4732e0000000p-3},{-0x1.f492200000000p-1,-0x1.ae4f1e0000000p-3},
{-0x1.f4e6040000000p-1,-0x1.a82a020000000p-3},{-0x1.f538b20000000p-1,-0x1.a203e20000000p-3},{-0x1.f58a2c0000000p-1,-0x1.9bdcc00000000p-3},{-0x1.f5da6e0000000p-1,-0x1.95b49e0000000p-3},
{-0x1.f6297c0000000p-1,-0x1.8f8b840000000p-3},{-0x1.f677560000000p-1,-0x1.8961720000000p-3},{-0x1.f6c3f80000000p-1,-0x1.83366e0000000p-3},{-0x1.f70f640000000p-1,-0x1.7d0a7c0000000p-3},
{-0x1.f7599a0000000p-1,-0x1.76dd9e0000000p-3},{-0x1.f7a29a0000000p-1,-0x1.70afd80000000p-3},{-0x1.f7ea620000000p-1,-0x1.6a81300000000p-3},{-0x1.f830f40000000p-1,-0x1.6451a80000000p-3},
{-0x1.f876500000000p-1,-0x1.5e21440000000p-3},{-0x1.f8ba740000000p-1,-0x1.57f0080000000p-3},{-0x1.f8fd600000000p-1,-0x1.51bdf80000000p-3},{-0x1.f93f140000000p-1,-0x1.4b8b180000000p-3},
{-0x1.f97f920000000p-1,-0x1.45576c0000000p-3},{-0x1.f9bed80000000p-1,-0x1.3f22f60000000p-3},{-0x1.f9fce60000000p-1,-0x1.38edbc0000000p-3},{-0x1.fa39ba0000000p-1,-0x1.32b7c00000000p-3},
{-0x1.fa75580000000p-1,-0x1.2c81060000000p-3},{-0x1.faafbc0000000p-1,-0x1.2649940000000p-3},{-0x1.fae8e80000000p-1,-0x1.20116e0000000p-3},{-0x1.fb20dc0000000p-1,-0x1.19d8940000000p-3},
{-0x1.fb57980000000p-1,-0x1.139f0c0000000p-3},{-0x1.fb8d180000000p-1,-0x1.0d64dc0000000p-3},{-0x1.fbc1620000000p-1,-0x1.072a040000000p-3},{-0x1.fbf4700000000p-1,-0x1.00ee8a0000000p-3},
{-0x1.fc26480000000p-1,-0x1.f564e60000000p-4},{-0x1.fc56e40000000p-1,-0x1.e8eb800000000p-4},{-0x1.fc86460000000p-1,-0x1.dc70ec0000000p-4},{-0x1.fcb4700000000p-1,-0x1.cff5340000000p-4},
{-0x1.fce1600000000p-1,-0x1.c3785c0000000p-4},{-0x1.fd0d160000000p-1,-0x1.b6fa6e0000000p-4},{-0x1.fd37920000000p-1,-0x1.aa7b720000000p-4},{-0x1.fd60d20000000p-1,-0x1.9dfb6e0000000p-4},
{-0x1.fd88da0000000p-1,-0x1.917a6c0000000p-4},{-0x1.fdafa80000000p-1,-0x1.84f8720000000p-4},{-0x1.fdd53a0000000p-1,-0x1.7875860000000p-4},{-0x1.fdf9920000000p-1,-0x1.6bf1b40000000p-4},
{-0x1.fe1cb00000000p-1,-0x1.5f6d000000000p-4},{-0x1.fe3e920000000p-1,-0x1.52e7740000000p-4},{-0x1.fe5f3a0000000p-1,-0x1.4661180000000p-4},{-0x1.fe7ea80000000p-1,-0x1.39d9f20000000p-4},
{-0x1.fe9cda0000000p-1,-0x1.2d520a0000000p-4},{-0x1.feb9d20000000p-1,-0x1.20c9680000000p-4},{-0x1.fed58e0000000p-1,-0x1.1440140000000p-4},{-0x1.fef0100000000p-1,-0x1.07b6140000000p-4},
{-0x1.ff09560000000p-1,-0x1.f656e80000000p-5},{-0x1.ff21620000000p-1,-0x1.dd40700000000p-5},{-0x1.ff38300000000p-1,-0x1.c428d20000000p-5},{-0x1.ff4dc60000000p-1,-0x1.ab101c0000000p-5},
{-0x1.ff621e0000000p-1,-0x1.91f6600000000p-5},{-0x1.ff753c0000000p-1,-0x1.78dbaa0000000p-5},{-0x1.ff871e0000000p-1,-0x1.5fc00e0000000p-5},{-0x1.ff97c40000000p-1,-0x1.46a3960000000p-5},
{-0x1.ffa72e0000000p-1,-0x1.2d86580000000p-5},{-0x1.ffb55e0000000p-1,-0x1.14685e0000000p-5},{-0x1.ffc2520000000p-1,-0x1.f693740000000p-6},{-0x1.ffce0a0000000p-1,-0x1.c454f40000000p-6},
{-0x1.ffd8860000000p-1,-0x1.9215600000000p-6},{-0x1.ffe1c60000000p-1,-0x1.5fd4d20000000p-6},{-0x1.ffe9cc0000000p-1,-0x1.2d936c0000000p-6},{-0x1.fff0940000000p-1,-0x1.f6a2960000000p-7},
{-0x1.fff6220000000p-1,-0x1.921d200000000p-7},{-0x1.fffa720000000p-1,-0x1.2d96b00000000p-7},{-0x1.fffd880000000p-1,-0x1.921f100000000p-8},{-0x1.ffff620000000p-1,-0x1.921f8c0000000p-9},
{-0x1.0000000000000p+0,-0x1.1a62640000000p-53},{-0x1.ffff620000000p-1,0x1.921f8c0000000p-9},{-0x1.fffd880000000p-1,0x1.921f100000000p-8},{-0x1.fffa720000000p-1,0x1.2d96b00000000p-7},
{-0x1.fff6220000000p-1,0x1.921d200000000p-7},{-0x1.fff0940000000p-1,0x1.f6a2960000000p-7},{-0x1.ffe9cc0000000p-1,0x1.2d936c0000000p-6},{-0x1.ffe1c60000000p-1,0x1.5fd4d20000000p-6},
{-0x1.ffd8860000000p-1,0x1.9215600000000p-6},{-0x1.ffce0a0000000p-1,0x1.c454f40000000p-6},{-0x1.ffc2520000000p-1,0x1.f693740000000p-6},{-0x1.ffb55e0000000p-1,0x1.14685e0000000p-5},
{-0x1.ffa72e0000000p-1,0x1.2d86580000000p-5},{-0x1.ff97c40000000p-1,0x1.46a3960000000p-5},{-0x1.ff871e0000000p-1,0x1.5fc00e0000000p-5},{-0x1.ff753c0000000p-1,0x1.78dbaa0000000p-5},
{-0x1.ff621e0000000p-1,0x1.91f6600000000p-5},{-0x1.ff4dc60000000p-1,0x1.ab101c0000000p-5},{-0x1.ff38300000000p-1,0x1.c428d20000000p-5},{-0x1.ff21620000000p-1,0x1.dd40700000000p-5},
{-0x1.ff09560000000p-1,0x1.f656e80000000p-5},{-0x1.fef0100000000p-1,0x1.07b6140000000p-4},{-0x1.fed58e0000000p-1,0x1.1440140000000p-4},{-0x1.feb9d20000000p-1,0x1.20c9680000000p-4},
{-0x1.fe9cda0000000p-1,0x1.2d520a0000000p-4},{-0x1.fe7ea80000000p-1,0x1.39d9f20000000p-4},{-0x1.fe5f3a0000000p-1,0x1.4661180000000p-4},{-0x1.fe3e920000000p-1,0x1.52e7740000000p-4},
{-0x1.fe1cb00000000p-1,0x1.5f6d000000000p-4},{-0x1.fdf9920000000p-1,0x1.6bf1b40000000p-4},{-0x1.fdd53a0000000p-1,0x1.7875860000000p-4},{-0x1.fdafa80000000p-1,0x1.84f8720000000p-4},
{-0x1.fd88da0000000p-1,0x1.917a6c0000000p-4},{-0x1.fd60d20000000p-1,0x1.9dfb6e0000000p-4},{-0x1.fd37920000000p-1,0x1.aa7b720000000p-4},{-0x1.fd0d160000000p-1,0x1.b6fa6e0000000p-4},
{-0x1.fce1600000000p-1,0x1.c3785c0000000p-4},{-0x1.fcb4700000000p-1,0x1.cff5340000000p-4},{-0x1.fc86460000000p-1,0x1.dc70ec0000000p-4},{-0x1.fc56e40000000p-1,0x1.e8eb800000000p-4},
{-0x1.fc26480000000p-1,0x1.f564e60000000p-4},{-0x1.fbf4700000000p-1,0x1.00ee8a0000000p-3},{-0x1.fbc1620000000p-1,0x1.072a040000000p-3},{-0x1.fb8d180000000p-1,0x1.0d64dc0000000p-3},
{-0x1.fb57980000000p-1,0x1.139f0c0000000p-3},{-0x1.fb20dc0000000p-1,0x1.19d8940000000p-3},{-0x1.fae8e80000000p-1,0x1.20116e0000000p-3},{-0x1.faafbc0000000p-1,0x1.2649940000000p-3},
{-0x1.fa75580000000p-1,0x1.2c81060000000p-3},{-0x1.fa39ba0000000p-1,0x1.32b7c00000000p-3},{-0x1.f9fce60000000p-1,0x1.38edbc0000000p-3},{-0x1.f9bed80000000p-1,0x1.3f22f60000000p-3},
{-0x1.f97f920000000p-1,0x1.45576c0000000p-3},{-0x1.f93f140000000p-1,0x1.4b8b180000000p-3},{-0x1.f8fd600000000p-1,0x1.51bdf80000000p-3},{-0x1.f8ba740000000p-1,0x1.57f0080000000p-3},
{-0x1.f876500000000p-1,0x1.5e21440000000p-3},{-0x1.f830f40000000p-1,0x1.6451a80000000p-3},{-0x1.f7ea620000000p-1,0x1.6a81300000000p-3},{-0x1.f7a29a0000000p-1,0x1.70afd80000000p-3},
{-0x1.f7599a0000000p-1,0x1.76dd9e0000000p-3},{-0x1.f70f640000000p-1,0x1.7d0a7c0000000p-3},{-0x1.f6c3f80000000p-1,0x1.83366e0000000p-3},{-0x1.f677560000000p-1,0x1.8961720000000p-3},
{-0x1.f6297c0000000p-1,0x1.8f8b840000000p-3},{-0x1.f5da6e0000000p-1,0x1.95b49e0000000p-3},{-0x1.f58a2c0000000p-1,0x1.9bdcc00000000p-3},{-0x1.f538b20000000p-1,0x1.a203e20000000p-3},
{-0x1.f4e6040000000p-1,0x1.a82a020000000p-3},{-0x1.f492200000000p-1,0x1.ae4f1e0000000p-3},{-0x1.f43d080000000p-1,0x1.b4732e0000000p-3},{-0x1.f3e6bc0000000p-1,0x1.ba96340000000p-3},
{-0x1.f38f3a0000000p-1,0x1.c0b8260000000p-3},{-0x1.f336860000000p-1,0x1.c6d9060000000p-3},{-0x1.f2dc9c0000000p-1,0x1.ccf8cc0000000p-3},{-0x1.f281800000000p-1,0x1.d317740000000p-3},
{-0x1.f225300000000p-1,0x1.d934fe0000000p-3},{-0x1.f1c7ac0000000p-1,0x1.df51640000000p-3},{-0x1.f168f60000000p-1,0x1.e56ca20000000p-3},{-0x1.f1090c0000000p-1,0x1.eb86b40000000p-3},
{-0x1.f0a7f00000000p-1,0x1.f19f980000000p-3},{-0x1.f045a20000000p-1,0x1.f7b7480000000p-3},{-0x1.efe2200000000p-1,0x1.fdcdc20000000p-3},{-0x1.ef7d6e0000000p-1,0x1.01f1800000000p-2},
{-0x1.ef178a0000000p-1,0x1.04fb800000000p-2},{-0x1.eeb0740000000p-1,0x1.0804e00000000p-2},{-0x1.ee482e0000000p-1,0x1.0b0d9c0000000p-2},{-0x1.eddeb60000000p-1,0x1.0e15b40000000p-2},
{-0x1.ed740e0000000p-1,0x1.111d260000000p-2},{-0x1.ed08360000000p-1,0x1.1423ee0000000p-2},{-0x1.ec9b2e0000000p-1,0x1.172a0e0000000p-2},{-0x1.ec2cf40000000p-1,0x1.1a2f800000000p-2},
{-0x1.ebbd8c0000000p-1,0x1.1d34440000000p-2},{-0x1.eb4cf60000000p-1,0x1.2038580000000p-2},{-0x1.eadb2e0000000p-1,0x1.233bba0000000p-2},{-0x1.ea683a0000000p-1,0x1.263e6a0000000p-2},
{-0x1.e9f4160000000p-1,0x1.2940620000000p-2},{-0x1.e97ec40000000p-1,0x1.2c41a40000000p-2},{-0x1.e908440000000p-1,0x1.2f422e0000000p-2},{-0x1.e890960000000p-1,0x1.3241fc0000000p-2},
{-0x1.e817ba0000000p-1,0x1.35410c0000000p-2},{-0x1.e79db20000000p-1,0x1.383f5e0000000p-2},{-0x1.e7227e0000000p-1,0x1.3b3cf00000000p-2},{-0x1.e6a61c0000000p-1,0x1.3e39be0000000p-2},
{-0x1.e6288e0000000p-1,0x1.4135ca0000000p-2},{-0x1.e5a9d60000000p-1,0x1.44310e0000000p-2},{-0x1.e529f00000000p-1,0x1.472b8a0000000p-2},{-0x1.e4a8e00000000p-1,0x1.4a253e0000000p-2},
{-0x1.e426a40000000p-1,0x1.4d1e240000000p-2},{-0x1.e3a33e0000000p-1,0x1.50163e0000000p-2},{-0x1.e31eae0000000p-1,0x1.530d880000000p-2},{-0x1.e298f40000000p-1,0x1.5604020000000p-2},
{-0x1.e212100000000p-1,0x1.58f9a80000000p-2},{-0x1.e18a020000000p-1,0x1.5bee780000000p-2},{-0x1.e100cc0000000p-1,0x1.5ee2740000000p-2},{-0x1.e0766e0000000p-1,0x1.61d5960000000p-2},
{-0x1.dfeae60000000p-1,0x1.64c7de0000000p-2},{-0x1.df5e360000000p-1,0x1.67b94a0000000p-2},{-0x1.ded0600000000p-1,0x1.6aa9d80000000p-2},{-0x1.de41600000000p-1,0x1.6d99860000000p-2},
{-0x1.ddb13c0000000p-1,0x1.7088540000000p-2},{-0x1.dd1ff00000000p-1,0x1.73763c0000000p-2},{-0x1.dc8d7c0000000p-1,0x1.7663400000000p-2},{-0x1.dbf9e40000000p-1,0x1.794f5e0000000p-2},
{-0x1.db65260000000p-1,0x1.7c3a940000000p-2},{-0x1.dacf420000000p-1,0x1.7f24de0000000p-2},{-0x1.da383a0000000p-1,0x1.820e3c0000000p-2},{-0x1.d9a00e0000000p-1,0x1.84f6aa0000000p-2},
{-0x1.d906bc0000000p-1,0x1.87de2a0000000p-2},{-0x1.d86c480000000p-1,0x1.8ac4b80000000p-2},{-0x1.d7d0b00000000p-1,0x1.8daa520000000p-2},{-0x1.d733f60000000p-1,0x1.908ef80000000p-2},
{-0x1.d696180000000p-1,0x1.9372a60000000p-2},{-0x1.d5f7180000000p-1,0x1.96555c0000000p-2},{-0x1.d556f60000000p-1,0x1.9937160000000p-2},{-0x1.d4b5b20000000p-1,0x1.9c17d40000000p-2},
{-0x1.d4134e0000000p-1,0x1.9ef7940000000p-2},{-0x1.d36fc80000000p-1,0x1.a1d6540000000p-2},{-0x1.d2cb220000000p-1,0x1.a4b4120000000p-2},{-0x1.d2255c0000000p-1,0x1.a790ce0000000p-2},
{-0x1.d17e780000000p-1,0x1.aa6c820000000p-2},{-0x1.d0d6720000000p-1,0x1.ad47320000000p-2},{-0x1.d02d500000000p-1,0x1.b020d60000000p-2},{-0x1.cf830e0000000p-1,0x1.b2f9720000000p-2},
{-0x1.ced7b00000000p-1,0x1.b5d1000000000p-2},{-0x1.ce2b320000000p-1,0x1.b8a7820000000p-2},{-0x1.cd7d980000000p-1,0x1.bb7cf20000000p-2},{-0x1.cccee20000000p-1,0x1.be51520000000p-2},
{-0x1.cc1f100000000p-1,0x1.c1249e0000000p-2},{-0x1.cb6e200000000p-1,0x1.c3f6d40000000p-2},{-0x1.cabc160000000p-1,0x1.c6c7f40000000p-2},{-0x1.ca08f20000000p-1,0x1.c997fc0000000p-2},
{-0x1.c954b20000000p-1,0x1.cc66ea0000000p-2},{-0x1.c89f580000000p-1,0x1.cf34ba0000000p-2},{-0x1.c7e8e60000000p-1,0x1.d2016e0000000p-2},{-0x1.c731580000000p-1,0x1.d4cd020000000p-2},
{-0x1.c678b40000000p-1,0x1.d797760000000p-2},{-0x1.c5bef60000000p-1,0x1.da60c60000000p-2},{-0x1.c504200000000p-1,0x1.dd28f20000000p-2},{-0x1.c448340000000p-1,0x1.dfeff60000000p-2},
{-0x1.c38b300000000p-1,0x1.e2b5d40000000p-2},{-0x1.c2cd140000000p-1,0x1.e57a860000000p-2},{-0x1.c20de40000000p-1,0x1.e83e0e0000000p-2},{-0x1.c14d9e0000000p-1,0x1.eb006a0000000p-2},
{-0x1.c08c420000000p-1,0x1.edc1960000000p-2},{-0x1.bfc9d20000000p-1,0x1.f081900000000p-2},{-0x1.bf064e0000000p-1,0x1.f3405a0000000p-2},{-0x1.be41b60000000p-1,0x1.f5fdee0000000p-2},
{-0x1.bd7c0a0000000p-1,0x1.f8ba4e0000000p-2},{-0x1.bcb54c0000000p-1,0x1.fb75760000000p-2},{-0x1.bbed7c0000000p-1,0x1.fe2f640000000p-2},{-0x1.bb249a0000000p-1,0x1.00740c0000000p-1},
{-0x1.ba5aa60000000p-1,0x1.01cfc80000000p-1},{-0x1.b98fa20000000p-1,0x1.032ae60000000p-1},{-0x1.b8c38e0000000p-1,0x1.0485620000000p-1},{-0x1.b7f6680000000p-1,0x1.05df3e0000000p-1},
{-0x1.b728340000000p-1,0x1.07387a0000000p-1},{-0x1.b658f20000000p-1,0x1.0891120000000p-1},{-0x1.b588a00000000p-1,0x1.09e9080000000p-1},{-0x1.b4b7400000000p-1,0x1.0b40580000000p-1},
{-0x1.b3e4d40000000p-1,0x1.0c97040000000p-1},{-0x1.b3115a0000000p-1,0x1.0ded0c0000000p-1},{-0x1.b23cd40000000p-1,0x1.0f426c0000000p-1},{-0x1.b167420000000p-1,0x1.1097240000000p-1},
{-0x1.b090a60000000p-1,0x1.11eb360000000p-1},{-0x1.afb8fe0000000p-1,0x1.133e9c0000000p-1},{-0x1.aee04c0000000p-1,0x1.14915a0000000p-1},{-0x1.ae06900000000p-1,0x1.15e36e0000000p-1},
{-0x1.ad2bca0000000p-1,0x1.1734d60000000p-1},{-0x1.ac4ffc0000000p-1,0x1.1885920000000p-1},{-0x1.ab73260000000p-1,0x1.19d5a00000000p-1},{-0x1.aa95480000000p-1,0x1.1b25020000000p-1},
{-0x1.a9b6620000000p-1,0x1.1c73b40000000p-1},{-0x1.a8d6760000000p-1,0x1.1dc1b60000000p-1},{-0x1.a7f5860000000p-1,0x1.1f0f080000000p-1},{-0x1.a7138e0000000p-1,0x1.205baa0000000p-1},
{-0x1.a630920000000p-1,0x1.21a79a0000000p-1},{-0x1.a54c920000000p-1,0x1.22f2d60000000p-1},{-0x1.a4678c0000000p-1,0x1.243d600000000p-1},{-0x1.a381840000000p-1,0x1.2587340000000p-1},
{-0x1.a29a7a0000000p-1,0x1.26d0540000000p-1},{-0x1.a1b26e0000000p-1,0x1.2818be0000000p-1},{-0x1.a0c95e0000000p-1,0x1.2960720000000p-1},{-0x1.9fdf500000000p-1,0x1.2aa76e0000000p-1},
{-0x1.9ef43e0000000p-1,0x1.2bedb20000000p-1},{-0x1.9e082e0000000p-1,0x1.2d333e0000000p-1},{-0x1.9d1b200000000p-1,0x1.2e780e0000000p-1},{-0x1.9c2d120000000p-1,0x1.2fbc240000000p-1},
{-0x1.9b3e040000000p-1,0x1.30ff800000000p-1},{-0x1.9a4dfa0000000p-1,0x1.32421e0000000p-1},{-0x1.995cf20000000p-1,0x1.3384000000000p-1},{-0x1.986af00000000p-1,0x1.34c5260000000p-1},
{-0x1.9777f00000000p-1,0x1.36058c0000000p-1},{-0x1.9683f40000000p-1,0x1.3745320000000p-1},{-0x1.958efe0000000p-1,0x1.3884180000000p-1},{-0x1.94990e0000000p-1,0x1.39c23e0000000p-1},
{-0x1.93a2240000000p-1,0x1.3affa20000000p-1},{-0x1.92aa420000000p-1,0x1.3c3c440000000p-1},{-0x1.91b1660000000p-1,0x1.3d78240000000p-1},{-0x1.90b7940000000p-1,0x1.3eb33e0000000p-1},
{-0x1.8fbcca0000000p-1,0x1.3fed960000000p-1},{-0x1.8ec10a0000000p-1,0x1.4127260000000p-1},{-0x1.8dc4540000000p-1,0x1.425ff20000000p-1},{-0x1.8cc6a80000000p-1,0x1.4397f60000000p-1},
{-0x1.8bc8060000000p-1,0x1.44cf320000000p-1},{-0x1.8ac8720000000p-1,0x1.4605a60000000p-1},{-0x1.89c7ea0000000p-1,0x1.473b520000000p-1},{-0x1.88c66e0000000p-1,0x1.4870340000000p-1},
{-0x1.87c4000000000p-1,0x1.49a44a0000000p-1},{-0x1.86c0a20000000p-1,0x1.4ad7960000000p-1},{-0x1.85bc520000000p-1,0x1.4c0a140000000p-1},{-0x1.84b7120000000p-1,0x1.4d3bc60000000p-1},
{-0x1.83b0e00000000p-1,0x1.4e6cac0000000p-1},{-0x1.82a9c20000000p-1,0x1.4f9cc20000000p-1},{-0x1.81a1b40000000p-1,0x1.50cc0a0000000p-1},{-0x1.8098b80000000p-1,0x1.51fa820000000p-1},
{-0x1.7f8ece0000000p-1,0x1.53282a0000000p-1},{-0x1.7e83f80000000p-1,0x1.5455000000000p-1},{-0x1.7d78360000000p-1,0x1.5581040000000p-1},{-0x1.7c6b8a0000000p-1,0x1.56ac360000000p-1},
{-0x1.7b5df20000000p-1,0x1.57d6940000000p-1},{-0x1.7a4f700000000p-1,0x1.59001e0000000p-1},{-0x1.7940060000000p-1,0x1.5a28d20000000p-1},{-0x1.782fb20000000p-1,0x1.5b50b20000000p-1},
{-0x1.771e760000000p-1,0x1.5c77bc0000000p-1},{-0x1.760c520000000p-1,0x1.5d9dee0000000p-1},{-0x1.74f9480000000p-1,0x1.5ec34a0000000p-1},{-0x1.73e5580000000p-1,0x1.5fe7cc0000000p-1},
{-0x1.72d0840000000p-1,0x1.610b760000000p-1},{-0x1.71baca0000000p-1,0x1.622e440000000p-1},{-0x1.70a42c0000000p-1,0x1.63503a0000000p-1},{-0x1.6f8caa0000000p-1,0x1.6471540000000p-1},
{-0x1.6e74460000000p-1,0x1.6591920000000p-1},{-0x1.6d5afe0000000p-1,0x1.66b0f40000000p-1},{-0x1.6c40d80000000p-1,0x1.67cf780000000p-1},{-0x1.6b25ce0000000p-1,0x1.68ed1e0000000p-1},
{-0x1.6a09e60000000p-1,0x1.6a09e60000000p-1},{-0x1.68ed1e0000000p-1,0x1.6b25ce0000000p-1},{-0x1.67cf780000000p-1,0x1.6c40d80000000p-1},{-0x1.66b0f40000000p-1,0x1.6d5afe0000000p-1},
{-0x1.6591920000000p-1,0x1.6e74460000000p-1},{-0x1.6471540000000p-1,0x1.6f8caa0000000p-1},{-0x1.63503a0000000p-1,0x1.70a42c0000000p-1},{-0x1.622e440000000p-1,0x1.71baca0000000p-1},
{-0x1.610b760000000p-1,0x1.72d0840000000p-1},{-0x1.5fe7cc0000000p-1,0x1.73e5580000000p-1},{-0x1.5ec34a0000000p-1,0x1.74f9480000000p-1},{-0x1.5d9dee0000000p-1,0x1.760c520000000p-1},
{-0x1.5c77bc0000000p-1,0x1.771e760000000p-1},{-0x1.5b50b20000000p-1,0x1.782fb20000000p-1},{-0x1.5a28d20000000p-1,0x1.7940060000000p-1},{-0x1.59001e0000000p-1,0x1.7a4f700000000p-1},
{-0x1.57d6940000000p-1,0x1.7b5df20000000p-1},{-0x1.56ac360000000p-1,0x1.7c6b8a0000000p-1},{-0x1.5581040000000p-1,0x1.7d78360000000p-1},{-0x1.5455000000000p-1,0x1.7e83f80000000p-1},
{-0x1.53282a0000000p-1,0x1.7f8ece0000000p-1},{-0x1.51fa820000000p-1,0x1.8098b80000000p-1},{-0x1.50cc0a0000000p-1,0x1.81a1b40000000p-1},{-0x1.4f9cc20000000p-1,0x1.82a9c20000000p-1},
{-0x1.4e6cac0000000p-1,0x1.83b0e00000000p-1},{-0x1.4d3bc60000000p-1,0x1.84b7120000000p-1},{-0x1.4c0a140000000p-1,0x1.85bc520000000p-1},{-0x1.4ad7960000000p-1,0x1.86c0a20000000p-1},
{-0x1.49a44a0000000p-1,0x1.87c4000000000p-1},{-0x1.4870340000000p-1,0x1.88c66e0000000p-1},{-0x1.473b520000000p-1,0x1.89c7ea0000000p-1},{-0x1.4605a60000000p-1,0x1.8ac8720000000p-1},
{-0x1.44cf320000000p-1,0x1.8bc8060000000p-1},{-0x1.4397f60000000p-1,0x1.8cc6a80000000p-1},{-0x1.425ff20000000p-1,0x1.8dc4540000000p-1},{-0x1.4127260000000p-1,0x1.8ec10a0000000p-1},
{-0x1.3fed960000000p-1,0x1.8fbcca0000000p-1},{-0x1.3eb33e0000000p-1,0x1.90b7940000000p-1},{-0x1.3d78240000000p-1,0x1.91b1660000000p-1},{-0x1.3c3c440000000p-1,0x1.92aa420000000p-1},
{-0x1.3affa20000000p-1,0x1.93a2240000000p-1},{-0x1.39c23e0000000p-1,0x1.94990e0000000p-1},{-0x1.3884180000000p-1,0x1.958efe0000000p-1},{-0x1.3745320000000p-1,0x1.9683f40000000p-1},
{-0x1.36058c0000000p-1,0x1.9777f00000000p-1},{-0x1.34c5260000000p-1,0x1.986af00000000p-1},{-0x1.3384000000000p-1,0x1.995cf20000000p-1},{-0x1.32421e0000000p-1,0x1.9a4dfa0000000p-1},
{-0x1.30ff800000000p-1,0x1.9b3e040000000p-1},{-0x1.2fbc240000000p-1,0x1.9c2d120000000p-1},{-0x1.2e780e0000000p-1,0x1.9d1b200000000p-1},{-0x1.2d333e0000000p-1,0x1.9e082e0000000p-1},
{-0x1.2bedb20000000p-1,0x1.9ef43e0000000p-1},{-0x1.2aa76e0000000p-1,0x1.9fdf500000000p-1},{-0x1.2960720000000p-1,0x1.a0c95e0000000p-1},{-0x1.2818be0000000p-1,0x1.a1b26e0000000p-1},
{-0x1.26d0540000000p-1,0x1.a29a7a0000000p-1},{-0x1.2587340000000p-1,0x1.a381840000000p-1},{-0x1.243d600000000p-1,0x1.a4678c0000000p-1},{-0x1.22f2d60000000p-1,0x1.a54c920000000p-1},
{-0x1.21a79a0000000p-1,0x1.a630920000000p-1},{-0x1.205baa0000000p-1,0x1.a7138e0000000p-1},{-0x1.1f0f080000000p-1,0x1.a7f5860000000p-1},{-0x1.1dc1b60000000p-1,0x1.a8d6760000000p-1},
{-0x1.1c73b40000000p-1,0x1.a9b6620000000p-1},{-0x1.1b25020000000p-1,0x1.aa95480000000p-1},{-0x1.19d5a00000000p-1,0x1.ab73260000000p-1},{-0x1.1885920000000p-1,0x1.ac4ffc0000000p-1},
{-0x1.1734d60000000p-1,0x1.ad2bca0000000p-1},{-0x1.15e36e0000000p-1,0x1.ae06900000000p-1},{-0x1.14915a0000000p-1,0x1.aee04c0000000p-1},{-0x1.133e9c0000000p-1,0x1.afb8fe0000000p-1},
{-0x1.11eb360000000p-1,0x1.b090a60000000p-1},{-0x1.1097240000000p-1,0x1.b167420000000p-1},{-0x1.0f426c0000000p-1,0x1.b23cd40000000p-1},{-0x1.0ded0c0000000p-1,0x1.b3115a0000000p-1},
{-0x1.0c97040000000p-1,0x1.b3e4d40000000p-1},{-0x1.0b40580000000p-1,0x1.b4b7400000000p-1},{-0x1.09e9080000000p-1,0x1.b588a00000000p-1},{-0x1.0891120000000p-1,0x1.b658f20000000p-1},
{-0x1.07387a0000000p-1,0x1.b728340000000p-1},{-0x1.05df3e0000000p-1,0x1.b7f6680000000p-1},{-0x1.0485620000000p-1,0x1.b8c38e0000000p-1},{-0x1.032ae60000000p-1,0x1.b98fa20000000p-1},
{-0x1.01cfc80000000p-1,0x1.ba5aa60000000p-1},{-0x1.00740c0000000p-1,0x1.bb249a0000000p-1},{-0x1.fe2f640000000p-2,0x1.bbed7c0000000p-1},{-0x1.fb75760000000p-2,0x1.bcb54c0000000p-1},
{-0x1.f8ba4e0000000p-2,0x1.bd7c0a0000000p-1},{-0x1.f5fdee0000000p-2,0x1.be41b60000000p-1},{-0x1.f3405a0000000p-2,0x1.bf064e0000000p-1},{-0x1.f081900000000p-2,0x1.bfc9d20000000p-1},
{-0x1.edc1960000000p-2,0x1.c08c420000000p-1},{-0x1.eb006a0000000p-2,0x1.c14d9e0000000p-1},{-0x1.e83e0e0000000p-2,0x1.c20de40000000p-1},{-0x1.e57a860000000p-2,0x1.c2cd140000000p-1},
{-0x1.e2b5d40000000p-2,0x1.c38b300000000p-1},{-0x1.dfeff60000000p-2,0x1.c448340000000p-1},{-0x1.dd28f20000000p-2,0x1.c504200000000p-1},{-0x1.da60c60000000p-2,0x1.c5bef60000000p-1},
{-0x1.d797760000000p-2,0x1.c678b40000000p-1},{-0x1.d4cd020000000p-2,0x1.c731580000000p-1},{-0x1.d2016e0000000p-2,0x1.c7e8e60000000p-1},{-0x1.cf34ba0000000p-2,0x1.c89f580000000p-1},
{-0x1.cc66ea0000000p-2,0x1.c954b20000000p-1},{-0x1.c997fc0000000p-2,0x1.ca08f20000000p-1},{-0x1.c6c7f40000000p-2,0x1.cabc160000000p-1},{-0x1.c3f6d40000000p-2,0x1.cb6e200000000p-1},
{-0x1.c1249e0000000p-2,0x1.cc1f100000000p-1},{-0x1.be51520000000p-2,0x1.cccee20000000p-1},{-0x1.bb7cf20000000p-2,0x1.cd7d980000000p-1},{-0x1.b8a7820000000p-2,0x1.ce2b320000000p-1},
{-0x1.b5d1000000000p-2,0x1.ced7b00000000p-1},{-0x1.b2f9720000000p-2,0x1.cf830e0000000p-1},{-0x1.b020d60000000p-2,0x1.d02d500000000p-1},{-0x1.ad47320000000p-2,0x1.d0d6720000000p-1},
{-0x1.aa6c820000000p-2,0x1.d17e780000000p-1},{-0x1.a790ce0000000p-2,0x1.d2255c0000000p-1},{-0x1.a4b4120000000p-2,0x1.d2cb220000000p-1},{-0x1.a1d6540000000p-2,0x1.d36fc80000000p-1},
{-0x1.9ef7940000000p-2,0x1.d4134e0000000p-1},{-0x1.9c17d40000000p-2,0x1.d4b5b20000000p-1},{-0x1.9937160000000p-2,0x1.d556f60000000p-1},{-0x1.96555c0000000p-2,0x1.d5f7180000000p-1},
{-0x1.9372a60000000p-2,0x1.d696180000000p-1},{-0x1.908ef80000000p-2,0x1.d733f60000000p-1},{-0x1.8daa520000000p-2,0x1.d7d0b00000000p-1},{-0x1.8ac4b80000000p-2,0x1.d86c480000000p-1},
{-0x1.87de2a0000000p-2,0x1.d906bc0000000p-1},{-0x1.84f6aa0000000p-2,0x1.d9a00e0000000p-1},{-0x1.820e3c0000000p-2,0x1.da383a0000000p-1},{-0x1.7f24de0000000p-2,0x1.dacf420000000p-1},
{-0x1.7c3a940000000p-2,0x1.db65260000000p-1},{-0x1.794f5e0000000p-2,0x1.dbf9e40000000p-1},{-0x1.7663400000000p-2,0x1.dc8d7c0000000p-1},{-0x1.73763c0000000p-2,0x1.dd1ff00000000p-1},
{-0x1.7088540000000p-2,0x1.ddb13c0000000p-1},{-0x1.6d99860000000p-2,0x1.de41600000000p-1},{-0x1.6aa9d80000000p-2,0x1.ded0600000000p-1},{-0x1.67b94a0000000p-2,0x1.df5e360000000p-1},
{-0x1.64c7de0000000p-2,0x1.dfeae60000000p-1},{-0x1.61d5960000000p-2,0x1.e0766e0000000p-1},{-0x1.5ee2740000000p-2,0x1.e100cc0000000p-1},{-0x1.5bee780000000p-2,0x1.e18a020000000p-1},
{-0x1.58f9a80000000p-2,0x1.e212100000000p-1},{-0x1.5604020000000p-2,0x1.e298f40000000p-1},{-0x1.530d880000000p-2,0x1.e31eae0000000p-1},{-0x1.50163e0000000p-2,0x1.e3a33e0000000p-1},
{-0x1.4d1e240000000p-2,0x1.e426a40000000p-1},{-0x1.4a253e0000000p-2,0x1.e4a8e00000000p-1},{-0x1.472b8a0000000p-2,0x1.e529f00000000p-1},{-0x1.44310e0000000p-2,0x1.e5a9d60000000p-1},
{-0x1.4135ca0000000p-2,0x1.e6288e0000000p-1},{-0x1.3e39be0000000p-2,0x1.e6a61c0000000p-1},{-0x1.3b3cf00000000p-2,0x1.e7227e0000000p-1},{-0x1.383f5e0000000p-2,0x1.e79db20000000p-1},
{-0x1.35410c0000000p-2,0x1.e817ba0000000p-1},{-0x1.3241fc0000000p-2,0x1.e890960000000p-1},{-0x1.2f422e0000000p-2,0x1.e908440000000p-1},{-0x1.2c41a40000000p-2,0x1.e97ec40000000p-1},
{-0x1.2940620000000p-2,0x1.e9f4160000000p-1},{-0x1.263e6a0000000p-2,0x1.ea683a0000000p-1},{-0x1.233bba0000000p-2,0x1.eadb2e0000000p-1},{-0x1.2038580000000p-2,0x1.eb4cf60000000p-1},
{-0x1.1d34440000000p-2,0x1.ebbd8c0000000p-1},{-0x1.1a2f800000000p-2,0x1.ec2cf40000000p-1},{-0x1.172a0e0000000p-2,0x1.ec9b2e0000000p-1},{-0x1.1423ee0000000p-2,0x1.ed08360000000p-1},
{-0x1.111d260000000p-2,0x1.ed740e0000000p-1},{-0x1.0e15b40000000p-2,0x1.eddeb60000000p-1},{-0x1.0b0d9c0000000p-2,0x1.ee482e0000000p-1},{-0x1.0804e00000000p-2,0x1.eeb0740000000p-1},
{-0x1.04fb800000000p-2,0x1.ef178a0000000p-1},{-0x1.01f1800000000p-2,0x1.ef7d6e0000000p-1},{-0x1.fdcdc20000000p-3,0x1.efe2200000000p-1},{-0x1.f7b7480000000p-3,0x1.f045a20000000p-1},
{-0x1.f19f980000000p-3,0x1.f0a7f00000000p-1},{-0x1.eb86b40000000p-3,0x1.f1090c0000000p-1},{-0x1.e56ca20000000p-3,0x1.f168f60000000p-1},{-0x1.df51640000000p-3,0x1.f1c7ac0000000p-1},
{-0x1.d934fe0000000p-3,0x1.f225300000000p-1},{-0x1.d317740000000p-3,0x1.f281800000000p-1},{-0x1.ccf8cc0000000p-3,0x1.f2dc9c0000000p-1},{-0x1.c6d9060000000p-3,0x1.f336860000000p-1},
{-0x1.c0b8260000000p-3,0x1.f38f3a0000000p-1},{-0x1.ba96340000000p-3,0x1.f3e6bc0000000p-1},{-0x1.b4732e0000000p-3,0x1.f43d080000000p-1},{-0x1.ae4f1e0000000p-3,0x1.f492200000000p-1},
{-0x1.a82a020000000p-3,0x1.f4e6040000000p-1},{-0x1.a203e20000000p-3,0x1.f538b20000000p-1},{-0x1.9bdcc00000000p-3,0x1.f58a2c0000000p-1},{-0x1.95b49e0000000p-3,0x1.f5da6e0000000p-1},
{-0x1.8f8b840000000p-3,0x1.f6297c0000000p-1},{-0x1.8961720000000p-3,0x1.f677560000000p-1},{-0x1.83366e0000000p-3,0x1.f6c3f80000000p-1},{-0x1.7d0a7c0000000p-3,0x1.f70f640000000p-1},
{-0x1.76dd9e0000000p-3,0x1.f7599a0000000p-1},{-0x1.70afd80000000p-3,0x1.f7a29a0000000p-1},{-0x1.6a81300000000p-3,0x1.f7ea620000000p-1},{-0x1.6451a80000000p-3,0x1.f830f40000000p-1},
{-0x1.5e21440000000p-3,0x1.f876500000000p-1},{-0x1.57f0080000000p-3,0x1.f8ba740000000p-1},{-0x1.51bdf80000000p-3,0x1.f8fd600000000p-1},{-0x1.4b8b180000000p-3,0x1.f93f140000000p-1},
{-0x1.45576c0000000p-3,0x1.f97f920000000p-1},{-0x1.3f22f60000000p-3,0x1.f9bed80000000p-1},{-0x1.38edbc0000000p-3,0x1.f9fce60000000p-1},{-0x1.32b7c00000000p-3,0x1.fa39ba0000000p-1},
{-0x1.2c81060000000p-3,0x1.fa75580000000p-1},{-0x1.2649940000000p-3,0x1.faafbc0000000p-1},{-0x1.20116e0000000p-3,0x1.fae8e80000000p-1},{-0x1.19d8940000000p-3,0x1.fb20dc0000000p-1},
{-0x1.139f0c0000000p-3,0x1.fb57980000000p-1},{-0x1.0d64dc0000000p-3,0x1.fb8d180000000p-1},{-0x1.072a040000000p-3,0x1.fbc1620000000p-1},{-0x1.00ee8a0000000p-3,0x1.fbf4700000000p-1},
{-0x1.f564e60000000p-4,0x1.fc26480000000p-1},{-0x1.e8eb800000000p-4,0x1.fc56e40000000p-1},{-0x1.dc70ec0000000p-4,0x1.fc86460000000p-1},{-0x1.cff5340000000p-4,0x1.fcb4700000000p-1},
{-0x1.c3785c0000000p-4,0x1.fce1600000000p-1},{-0x1.b6fa6e0000000p-4,0x1.fd0d160000000p-1},{-0x1.aa7b720000000p-4,0x1.fd37920000000p-1},{-0x1.9dfb6e0000000p-4,0x1.fd60d20000000p-1},
{-0x1.917a6c0000000p-4,0x1.fd88da0000000p-1},{-0x1.84f8720000000p-4,0x1.fdafa80000000p-1},{-0x1.7875860000000p-4,0x1.fdd53a0000000p-1},{-0x1.6bf1b40000000p-4,0x1.fdf9920000000p-1},
{-0x1.5f6d000000000p-4,0x1.fe1cb00000000p-1},{-0x1.52e7740000000p-4,0x1.fe3e920000000p-1},{-0x1.4661180000000p-4,0x1.fe5f3a0000000p-1},{-0x1.39d9f20000000p-4,0x1.fe7ea80000000p-1},
{-0x1.2d520a0000000p-4,0x1.fe9cda0000000p-1},{-0x1.20c9680000000p-4,0x1.feb9d20000000p-1},{-0x1.1440140000000p-4,0x1.fed58e0000000p-1},{-0x1.07b6140000000p-4,0x1.fef0100000000p-1},
{-0x1.f656e80000000p-5,0x1.ff09560000000p-1},{-0x1.dd40700000000p-5,0x1.ff21620000000p-1},{-0x1.c428d20000000p-5,0x1.ff38300000000p-1},{-0x1.ab101c0000000p-5,0x1.ff4dc60000000p-1},
{-0x1.91f6600000000p-5,0x1.ff621e0000000p-1},{-0x1.78dbaa0000000p-5,0x1.ff753c0000000p-1},{-0x1.5fc00e0000000p-5,0x1.ff871e0000000p-1},{-0x1.46a3960000000p-5,0x1.ff97c40000000p-1},
{-0x1.2d86580000000p-5,0x1.ffa72e0000000p-1},{-0x1.14685e0000000p-5,0x1.ffb55e0000000p-1},{-0x1.f693740000000p-6,0x1.ffc2520000000p-1},{-0x1.c454f40000000p-6,0x1.ffce0a0000000p-1},
{-0x1.9215600000000p-6,0x1.ffd8860000000p-1},{-0x1.5fd4d20000000p-6,0x1.ffe1c60000000p-1},{-0x1.2d936c0000000p-6,0x1.ffe9cc0000000p-1},{-0x1.f6a2960000000p-7,0x1.fff0940000000p-1},
{-0x1.921d200000000p-7,0x1.fff6220000000p-1},{-0x1.2d96b00000000p-7,0x1.fffa720000000p-1},{-0x1.921f100000000p-8,0x1.fffd880000000p-1},{-0x1.921f8c0000000p-9,0x1.ffff620000000p-1},
{-0x1.a793940000000p-53,0x1.0000000000000p+0},{0x1.921f8c0000000p-9,0x1.ffff620000000p-1},{0x1.921f100000000p-8,0x1.fffd880000000p-1},{0x1.2d96b00000000p-7,0x1.fffa720000000p-1},
{0x1.921d200000000p-7,0x1.fff6220000000p-1},{0x1.f6a2960000000p-7,0x1.fff0940000000p-1},{0x1.2d936c0000000p-6,0x1.ffe9cc0000000p-1},{0x1.5fd4d20000000p-6,0x1.ffe1c60000000p-1},
{0x1.9215600000000p-6,0x1.ffd8860000000p-1},{0x1.c454f40000000p-6,0x1.ffce0a0000000p-1},{0x1.f693740000000p-6,0x1.ffc2520000000p-1},{0x1.14685e0000000p-5,0x1.ffb55e0000000p-1},
{0x1.2d86580000000p-5,0x1.ffa72e0000000p-1},{0x1.46a3960000000p-5,0x1.ff97c40000000p-1},{0x1.5fc00e0000000p-5,0x1.ff871e0000000p-1},{0x1.78dbaa0000000p-5,0x1.ff753c0000000p-1},
{0x1.91f6600000000p-5,0x1.ff621e0000000p-1},{0x1.ab101c0000000p-5,0x1.ff4dc60000000p-1},{0x1.c428d20000000p-5,0x1.ff38300000000p-1},{0x1.dd40700000000p-5,0x1.ff21620000000p-1},
{0x1.f656e80000000p-5,0x1.ff09560000000p-1},{0x1.07b6140000000p-4,0x1.fef0100000000p-1},{0x1.1440140000000p-4,0x1.fed58e0000000p-1},{0x1.20c9680000000p-4,0x1.feb9d20000000p-1},
{0x1.2d520a0000000p-4,0x1.fe9cda0000000p-1},{0x1.39d9f20000000p-4,0x1.fe7ea80000000p-1},{0x1.4661180000000p-4,0x1.fe5f3a0000000p-1},{0x1.52e7740000000p-4,0x1.fe3e920000000p-1},
{0x1.5f6d000000000p-4,0x1.fe1cb00000000p-1},{0x1.6bf1b40000000p-4,0x1.fdf9920000000p-1},{0x1.7875860000000p-4,0x1.fdd53a0000000p-1},{0x1.84f8720000000p-4,0x1.fdafa80000000p-1},
{0x1.917a6c0000000p-4,0x1.fd88da0000000p-1},{0x1.9dfb6e0000000p-4,0x1.fd60d20000000p-1},{0x1.aa7b720000000p-4,0x1.fd37920000000p-1},{0x1.b6fa6e0000000p-4,0x1.fd0d160000000p-1},
{0x1.c3785c0000000p-4,0x1.fce1600000000p-1},{0x1.cff5340000000p-4,0x1.fcb4700000000p-1},{0x1.dc70ec0000000p-4,0x1.fc86460000000p-1},{0x1.e8eb800000000p-4,0x1.fc56e40000000p-1},
{0x1.f564e60000000p-4,0x1.fc26480000000p-1},{0x1.00ee8a0000000p-3,0x1.fbf4700000000p-1},{0x1.072a040000000p-3,0x1.fbc1620000000p-1},{0x1.0d64dc0000000p-3,0x1.fb8d180000000p-1},
{0x1.139f0c0000000p-3,0x1.fb57980000000p-1},{0x1.19d8940000000p-3,0x1.fb20dc0000000p-1},{0x1.20116e0000000p-3,0x1.fae8e80000000p-1},{0x1.2649940000000p-3,0x1.faafbc0000000p-1},
{0x1.2c81060000000p-3,0x1.fa75580000000p-1},{0x1.32b7c00000000p-3,0x1.fa39ba0000000p-1},{0x1.38edbc0000000p-3,0x1.f9fce60000000p-1},{0x1.3f22f60000000p-3,0x1.f9bed80000000p-1},
{0x1.45576c0000000p-3,0x1.f97f920000000p-1},{0x1.4b8b180000000p-3,0x1.f93f140000000p-1},{0x1.51bdf80000000p-3,0x1.f8fd600000000p-1},{0x1.57f0080000000p-3,0x1.f8ba740000000p-1},
{0x1.5e21440000000p-3,0x1.f876500000000p-1},{0x1.6451a80000000p-3,0x1.f830f40000000p-1},{0x1.6a81300000000p-3,0x1.f7ea620000000p-1},{0x1.70afd80000000p-3,0x1.f7a29a0000000p-1},
{0x1.76dd9e0000000p-3,0x1.f7599a0000000p-1},{0x1.7d0a7c0000000p-3,0x1.f70f640000000p-1},{0x1.83366e0000000p-3,0x1.f6c3f80000000p-1},{0x1.8961720000000p-3,0x1.f677560000000p-1},
{0x1.8f8b840000000p-3,0x1.f6297c0000000p-1},{0x1.95b49e0000000p-3,0x1.f5da6e0000000p-1},{0x1.9bdcc00000000p-3,0x1.f58a2c0000000p-1},{0x1.a203e20000000p-3,0x1.f538b20000000p-1},
{0x1.a82a020000000p-3,0x1.f4e6040000000p-1},{0x1.ae4f1e0000000p-3,0x1.f492200000000p-1},{0x1.b4732e0000000p-3,0x1.f43d080000000p-1},{0x1.ba96340000000p-3,0x1.f3e6bc0000000p-1},
{0x1.c0b8260000000p-3,0x1.f38f3a0000000p-1},{0x1.c6d9060000000p-3,0x1.f336860000000p-1},{0x1.ccf8cc0000000p-3,0x1.f2dc9c0000000p-1},{0x1.d317740000000p-3,0x1.f281800000000p-1},
{0x1.d934fe0000000p-3,0x1.f225300000000p-1},{0x1.df51640000000p-3,0x1.f1c7ac0000000p-1},{0x1.e56ca20000000p-3,0x1.f168f60000000p-1},{0x1.eb86b40000000p-3,0x1.f1090c0000000p-1},
{0x1.f19f980000000p-3,0x1.f0a7f00000000p-1},{0x1.f7b7480000000p-3,0x1.f045a20000000p-1},{0x1.fdcdc20000000p-3,0x1.efe2200000000p-1},{0x1.01f1800000000p-2,0x1.ef7d6e0000000p-1},
{0x1.04fb800000000p-2,0x1.ef178a0000000p-1},{0x1.0804e00000000p-2,0x1.eeb0740000000p-1},{0x1.0b0d9c0000000p-2,0x1.ee482e0000000p-1},{0x1.0e15b40000000p-2,0x1.eddeb60000000p-1},
{0x1.111d260000000p-2,0x1.ed740e0000000p-1},{0x1.1423ee0000000p-2,0x1.ed08360000000p-1},{0x1.172a0e0000000p-2,0x1.ec9b2e0000000p-1},{0x1.1a2f800000000p-2,0x1.ec2cf40000000p-1},
{0x1.1d34440000000p-2,0x1.ebbd8c0000000p-1},{0x1.2038580000000p-2,0x1.eb4cf60000000p-1},{0x1.233bba0000000p-2,0x1.eadb2e0000000p-1},{0x1.263e6a0000000p-2,0x1.ea683a0000000p-1},
{0x1.2940620000000p-2,0x1.e9f4160000000p-1},{0x1.2c41a40000000p-2,0x1.e97ec40000000p-1},{0x1.2f422e0000000p-2,0x1.e908440000000p-1},{0x1.3241fc0000000p-2,0x1.e890960000000p-1},
{0x1.35410c0000000p-2,0x1.e817ba0000000p-1},{0x1.383f5e0000000p-2,0x1.e79db20000000p-1},{0x1.3b3cf00000000p-2,0x1.e7227e0000000p-1},{0x1.3e39be0000000p-2,0x1.e6a61c0000000p-1},
{0x1.4135ca0000000p-2,0x1.e6288e0000000p-1},{0x1.44310e0000000p-2,0x1.e5a9d60000000p-1},{0x1.472b8a0000000p-2,0x1.e529f00000000p-1},{0x1.4a253e0000000p-2,0x1.e4a8e00000000p-1},
{0x1.4d1e240000000p-2,0x1.e426a40000000p-1},{0x1.50163e0000000p-2,0x1.e3a33e0000000p-1},{0x1.530d880000000p-2,0x1.e31eae0000000p-1},{0x1.5604020000000p-2,0x1.e298f40000000p-1},
{0x1.58f9a80000000p-2,0x1.e212100000000p-1},{0x1.5bee780000000p-2,0x1.e18a020000000p-1},{0x1.5ee2740000000p-2,0x1.e100cc0000000p-1},{0x1.61d5960000000p-2,0x1.e0766e0000000p-1},
{0x1.64c7de0000000p-2,0x1.dfeae60000000p-1},{0x1.67b94a0000000p-2,0x1.df5e360000000p-1},{0x1.6aa9d80000000p-2,0x1.ded0600000000p-1},{0x1.6d99860000000p-2,0x1.de41600000000p-1},
{0x1.7088540000000p-2,0x1.ddb13c0000000p-1},{0x1.73763c0000000p-2,0x1.dd1ff00000000p-1},{0x1.7663400000000p-2,0x1.dc8d7c0000000p-1},{0x1.794f5e0000000p-2,0x1.dbf9e40000000p-1},
{0x1.7c3a940000000p-2,0x1.db65260000000p-1},{0x1.7f24de0000000p-2,0x1.dacf420000000p-1},{0x1.820e3c0000000p-2,0x1.da383a0000000p-1},{0x1.84f6aa0000000p-2,0x1.d9a00e0000000p-1},
{0x1.87de2a0000000p-2,0x1.d906bc0000000p-1},{0x1.8ac4b80000000p-2,0x1.d86c480000000p-1},{0x1.8daa520000000p-2,0x1.d7d0b00000000p-1},{0x1.908ef80000000p-2,0x1.d733f60000000p-1},
{0x1.9372a60000000p-2,0x1.d696180000000p-1},{0x1.96555c0000000p-2,0x1.d5f7180000000p-1},{0x1.9937160000000p-2,0x1.d556f60000000p-1},{0x1.9c17d40000000p-2,0x1.d4b5b20000000p-1},
{0x1.9ef7940000000p-2,0x1.d4134e0000000p-1},{0x1.a1d6540000000p-2,0x1.d36fc80000000p-1},{0x1.a4b4120000000p-2,0x1.d2cb220000000p-1},{0x1.a790ce0000000p-2,0x1.d2255c0000000p-1},
{0x1.aa6c820000000p-2,0x1.d17e780000000p-1},{0x1.ad47320000000p-2,0x1.d0d6720000000p-1},{0x1.b020d60000000p-2,0x1.d02d500000000p-1},{0x1.b2f9720000000p-2,0x1.cf830e0000000p-1},
{0x1.b5d1000000000p-2,0x1.ced7b00000000p-1},{0x1.b8a7820000000p-2,0x1.ce2b320000000p-1},{0x1.bb7cf20000000p-2,0x1.cd7d980000000p-1},{0x1.be51520000000p-2,0x1.cccee20000000p-1},
{0x1.c1249e0000000p-2,0x1.cc1f100000000p-1},{0x1.c3f6d40000000p-2,0x1.cb6e200000000p-1},{0x1.c6c7f40000000p-2,0x1.cabc160000000p-1},{0x1.c997fc0000000p-2,0x1.ca08f20000000p-1},
{0x1.cc66ea0000000p-2,0x1.c954b20000000p-1},{0x1.cf34ba0000000p-2,0x1.c89f580000000p-1},{0x1.d2016e0000000p-2,0x1.c7e8e60000000p-1},{0x1.d4cd020000000p-2,0x1.c731580000000p-1},
{0x1.d797760000000p-2,0x1.c678b40000000p-1},{0x1.da60c60000000p-2,0x1.c5bef60000000p-1},{0x1.dd28f20000000p-2,0x1.c504200000000p-1},{0x1.dfeff60000000p-2,0x1.c448340000000p-1},
{0x1.e2b5d40000000p-2,0x1.c38b300000000p-1},{0x1.e57a860000000p-2,0x1.c2cd140000000p-1},{0x1.e83e0e0000000p-2,0x1.c20de40000000p-1},{0x1.eb006a0000000p-2,0x1.c14d9e0000000p-1},
{0x1.edc1960000000p-2,0x1.c08c420000000p-1},{0x1.f081900000000p-2,0x1.bfc9d20000000p-1},{0x1.f3405a0000000p-2,0x1.bf064e0000000p-1},{0x1.f5fdee0000000p-2,0x1.be41b60000000p-1},
{0x1.f8ba4e0000000p-2,0x1.bd7c0a0000000p-1},{0x1.fb75760000000p-2,0x1.bcb54c0000000p-1},{0x1.fe2f640000000p-2,0x1.bbed7c0000000p-1},{0x1.00740c0000000p-1,0x1.bb249a0000000p-1},
{0x1.01cfc80000000p-1,0x1.ba5aa60000000p-1},{0x1.032ae60000000p-1,0x1.b98fa20000000p-1},{0x1.0485620000000p-1,0x1.b8c38e0000000p-1},{0x1.05df3e0000000p-1,0x1.b7f6680000000p-1},
{0x1.07387a0000000p-1,0x1.b728340000000p-1},{0x1.0891120000000p-1,0x1.b658f20000000p-1},{0x1.09e9080000000p-1,0x1.b588a00000000p-1},{0x1.0b40580000000p-1,0x1.b4b7400000000p-1},
{0x1.0c97040000000p-1,0x1.b3e4d40000000p-1},{0x1.0ded0c0000000p-1,0x1.b3115a0000000p-1},{0x1.0f426c0000000p-1,0x1.b23cd40000000p-1},{0x1.1097240000000p-1,0x1.b167420000000p-1},
{0x1.11eb360000000p-1,0x1.b090a60000000p-1},{0x1.133e9c0000000p-1,0x1.afb8fe0000000p-1},{0x1.14915a0000000p-1,0x1.aee04c0000000p-1},{0x1.15e36e0000000p-1,0x1.ae06900000000p-1},
{0x1.1734d60000000p-1,0x1.ad2bca0000000p-1},{0x1.1885920000000p-1,0x1.ac4ffc0000000p-1},{0x1.19d5a00000000p-1,0x1.ab73260000000p-1},{0x1.1b25020000000p-1,0x1.aa95480000000p-1},
{0x1.1c73b40000000p-1,0x1.a9b6620000000p-1},{0x1.1dc1b60000000p-1,0x1.a8d6760000000p-1},{0x1.1f0f080000000p-1,0x1.a7f5860000000p-1},{0x1.205baa0000000p-1,0x1.a7138e0000000p-1},
{0x1.21a79a0000000p-1,0x1.a630920000000p-1},{0x1.22f2d60000000p-1,0x1.a54c920000000p-1},{0x1.243d600000000p-1,0x1.a4678c0000000p-1},{0x1.2587340000000p-1,0x1.a381840000000p-1},
{0x1.26d0540000000p-1,0x1.a29a7a0000000p-1},{0x1.2818be0000000p-1,0x1.a1b26e0000000p-1},{0x1.2960720000000p-1,0x1.a0c95e0000000p-1},{0x1.2aa76e0000000p-1,0x1.9fdf500000000p-1},
{0x1.2bedb20000000p-1,0x1.9ef43e0000000p-1},{0x1.2d333e0000000p-1,0x1.9e082e0000000p-1},{0x1.2e780e0000000p-1,0x1.9d1b200000000p-1},{0x1.2fbc240000000p-1,0x1.9c2d120000000p-1},
{0x1.30ff800000000p-1,0x1.9b3e040000000p-1},{0x1.32421e0000000p-1,0x1.9a4dfa0000000p-1},{0x1.3384000000000p-1,0x1.995cf20000000p-1},{0x1.34c5260000000p-1,0x1.986af00000000p-1},
{0x1.36058c0000000p-1,0x1.9777f00000000p-1},{0x1.3745320000000p-1,0x1.9683f40000000p-1},{0x1.3884180000000p-1,0x1.958efe0000000p-1},{0x1.39c23e0000000p-1,0x1.94990e0000000p-1},
{0x1.3affa20000000p-1,0x1.93a2240000000p-1},{0x1.3c3c440000000p-1,0x1.92aa420000000p-1},{0x1.3d78240000000p-1,0x1.91b1660000000p-1},{0x1.3eb33e0000000p-1,0x1.90b7940000000p-1},
{0x1.3fed960000000p-1,0x1.8fbcca0000000p-1},{0x1.4127260000000p-1,0x1.8ec10a0000000p-1},{0x1.425ff20000000p-1,0x1.8dc4540000000p-1},{0x1.4397f60000000p-1,0x1.8cc6a80000000p-1},
{0x1.44cf320000000p-1,0x1.8bc8060000000p-1},{0x1.4605a60000000p-1,0x1.8ac8720000000p-1},{0x1.473b520000000p-1,0x1.89c7ea0000000p-1},{0x1.4870340000000p-1,0x1.88c66e0000000p-1},
{0x1.49a44a0000000p-1,0x1.87c4000000000p-1},{0x1.4ad7960000000p-1,0x1.86c0a20000000p-1},{0x1.4c0a140000000p-1,0x1.85bc520000000p-1},{0x1.4d3bc60000000p-1,0x1.84b7120000000p-1},
{0x1.4e6cac0000000p-1,0x1.83b0e00000000p-1},{0x1.4f9cc20000000p-1,0x1.82a9c20000000p-1},{0x1.50cc0a0000000p-1,0x1.81a1b40000000p-1},{0x1.51fa820000000p-1,0x1.8098b80000000p-1},
{0x1.53282a0000000p-1,0x1.7f8ece0000000p-1},{0x1.5455000000000p-1,0x1.7e83f80000000p-1},{0x1.5581040000000p-1,0x1.7d78360000000p-1},{0x1.56ac360000000p-1,0x1.7c6b8a0000000p-1},
{0x1.57d6940000000p-1,0x1.7b5df20000000p-1},{0x1.59001e0000000p-1,0x1.7a4f700000000p-1},{0x1.5a28d20000000p-1,0x1.7940060000000p-1},{0x1.5b50b20000000p-1,0x1.782fb20000000p-1},
{0x1.5c77bc0000000p-1,0x1.771e760000000p-1},{0x1.5d9dee0000000p-1,0x1.760c520000000p-1},{0x1.5ec34a0000000p-1,0x1.74f9480000000p-1},{0x1.5fe7cc0000000p-1,0x1.73e5580000000p-1},
{0x1.610b760000000p-1,0x1.72d0840000000p-1},{0x1.622e440000000p-1,0x1.71baca0000000p-1},{0x1.63503a0000000p-1,0x1.70a42c0000000p-1},{0x1.6471540000000p-1,0x1.6f8caa0000000p-1},
{0x1.6591920000000p-1,0x1.6e74460000000p-1},{0x1.66b0f40000000p-1,0x1.6d5afe0000000p-1},{0x1.67cf780000000p-1,0x1.6c40d80000000p-1},{0x1.68ed1e0000000p-1,0x1.6b25ce0000000p-1},
{0x1.6a09e60000000p-1,0x1.6a09e60000000p-1},{0x1.6b25ce0000000p-1,0x1.68ed1e0000000p-1},{0x1.6c40d80000000p-1,0x1.67cf780000000p-1},{0x1.6d5afe0000000p-1,0x1.66b0f40000000p-1},
{0x1.6e74460000000p-1,0x1.6591920000000p-1},{0x1.6f8caa0000000p-1,0x1.6471540000000p-1},{0x1.70a42c0000000p-1,0x1.63503a0000000p-1},{0x1.71baca0000000p-1,0x1.622e440000000p-1},
{0x1.72d0840000000p-1,0x1.610b760000000p-1},{0x1.73e5580000000p-1,0x1.5fe7cc0000000p-1},{0x1.74f9480000000p-1,0x1.5ec34a0000000p-1},{0x1.760c520000000p-1,0x1.5d9dee0000000p-1},
{0x1.771e760000000p-1,0x1.5c77bc0000000p-1},{0x1.782fb20000000p-1,0x1.5b50b20000000p-1},{0x1.7940060000000p-1,0x1.5a28d20000000p-1},{0x1.7a4f700000000p-1,0x1.59001e0000000p-1},
{0x1.7b5df20000000p-1,0x1.57d6940000000p-1},{0x1.7c6b8a0000000p-1,0x1.56ac360000000p-1},{0x1.7d78360000000p-1,0x1.5581040000000p-1},{0x1.7e83f80000000p-1,0x1.5455000000000p-1},
{0x1.7f8ece0000000p-1,0x1.53282a0000000p-1},{0x1.8098b80000000p-1,0x1.51fa820000000p-1},{0x1.81a1b40000000p-1,0x1.50cc0a0000000p-1},{0x1.82a9c20000000p-1,0x1.4f9cc20000000p-1},
{0x1.83b0e00000000p-1,0x1.4e6cac0000000p-1},{0x1.84b7120000000p-1,0x1.4d3bc60000000p-1},{0x1.85bc520000000p-1,0x1.4c0a140000000p-1},{0x1.86c0a20000000p-1,0x1.4ad7960000000p-1},
{0x1.87c4000000000p-1,0x1.49a44a0000000p-1},{0x1.88c66e0000000p-1,0x1.4870340000000p-1},{0x1.89c7ea0000000p-1,0x1.473b520000000p-1},{0x1.8ac8720000000p-1,0x1.4605a60000000p-1},
{0x1.8bc8060000000p-1,0x1.44cf320000000p-1},{0x1.8cc6a80000000p-1,0x1.4397f60000000p-1},{0x1.8dc4540000000p-1,0x1.425ff20000000p-1},{0x1.8ec10a0000000p-1,0x1.4127260000000p-1},
{0x1.8fbcca0000000p-1,0x1.3fed960000000p-1},{0x1.90b7940000000p-1,0x1.3eb33e0000000p-1},{0x1.91b1660000000p-1,0x1.3d78240000000p-1},{0x1.92aa420000000p-1,0x1.3c3c440000000p-1},
{0x1.93a2240000000p-1,0x1.3affa20000000p-1},{0x1.94990e0000000p-1,0x1.39c23e0000000p-1},{0x1.958efe0000000p-1,0x1.3884180000000p-1},{0x1.9683f40000000p-1,0x1.3745320000000p-1},
{0x1.9777f00000000p-1,0x1.36058c0000000p-1},{0x1.986af00000000p-1,0x1.34c5260000000p-1},{0x1.995cf20000000p-1,0x1.3384000000000p-1},{0x1.9a4dfa0000000p-1,0x1.32421e0000000p-1},
{0x1.9b3e040000000p-1,0x1.30ff800000000p-1},{0x1.9c2d120000000p-1,0x1.2fbc240000000p-1},{0x1.9d1b200000000p-1,0x1.2e780e0000000p-1},{0x1.9e082e0000000p-1,0x1.2d333e0000000p-1},
{0x1.9ef43e0000000p-1,0x1.2bedb20000000p-1},{0x1.9fdf500000000p-1,0x1.2aa76e0000000p-1},{0x1.a0c95e0000000p-1,0x1.2960720000000p-1},{0x1.a1b26e0000000p-1,0x1.2818be0000000p-1},
{0x1.a29a7a0000000p-1,0x1.26d0540000000p-1},{0x1.a381840000000p-1,0x1.2587340000000p-1},{0x1.a4678c0000000p-1,0x1.243d600000000p-1},{0x1.a54c920000000p-1,0x1.22f2d60000000p-1},
{0x1.a630920000000p-1,0x1.21a79a0000000p-1},{0x1.a7138e0000000p-1,0x1.205baa0000000p-1},{0x1.a7f5860000000p-1,0x1.1f0f080000000p-1},{0x1.a8d6760000000p-1,0x1.1dc1b60000000p-1},
{0x1.a9b6620000000p-1,0x1.1c73b40000000p-1},{0x1.aa95480000000p-1,0x1.1b25020000000p-1},{0x1.ab73260000000p-1,0x1.19d5a00000000p-1},{0x1.ac4ffc0000000p-1,0x1.1885920000000p-1},
{0x1.ad2bca0000000p-1,0x1.1734d60000000p-1},{0x1.ae06900000000p-1,0x1.15e36e0000000p-1},{0x1.aee04c0000000p-1,0x1.14915a0000000p-1},{0x1.afb8fe0000000p-1,0x1.133e9c0000000p-1},
{0x1.b090a60000000p-1,0x1.11eb360000000p-1},{0x1.b167420000000p-1,0x1.1097240000000p-1},{0x1.b23cd40000000p-1,0x1.0f426c0000000p-1},{0x1.b3115a0000000p-1,0x1.0ded0c0000000p-1},
{0x1.b3e4d40000000p-1,0x1.0c97040000000p-1},{0x1.b4b7400000000p-1,0x1.0b40580000000p-1},{0x1.b588a00000000p-1,0x1.09e9080000000p-1},{0x1.b658f20000000p-1,0x1.0891120000000p-1},
{0x1.b728340000000p-1,0x1.07387a0000000p-1},{0x1.b7f6680000000p-1,0x1.05df3e0000000p-1},{0x1.b8c38e0000000p-1,0x1.0485620000000p-1},{0x1.b98fa20000000p-1,0x1.032ae60000000p-1},
{0x1.ba5aa60000000p-1,0x1.01cfc80000000p-1},{0x1.bb249a0000000p-1,0x1.00740c0000000p-1},{0x1.bbed7c0000000p-1,0x1.fe2f640000000p-2},{0x1.bcb54c0000000p-1,0x1.fb75760000000p-2},
{0x1.bd7c0a0000000p-1,0x1.f8ba4e0000000p-2},{0x1.be41b60000000p-1,0x1.f5fdee0000000p-2},{0x1.bf064e0000000p-1,0x1.f3405a0000000p-2},{0x1.bfc9d20000000p-1,0x1.f081900000000p-2},
{0x1.c08c420000000p-1,0x1.edc1960000000p-2},{0x1.c14d9e0000000p-1,0x1.eb006a0000000p-2},{0x1.c20de40000000p-1,0x1.e83e0e0000000p-2},{0x1.c2cd140000000p-1,0x1.e57a860000000p-2},
{0x1.c38b300000000p-1,0x1.e2b5d40000000p-2},{0x1.c448340000000p-1,0x1.dfeff60000000p-2},{0x1.c504200000000p-1,0x1.dd28f20000000p-2},{0x1.c5bef60000000p-1,0x1.da60c60000000p-2},
{0x1.c678b40000000p-1,0x1.d797760000000p-2},{0x1.c731580000000p-1,0x1.d4cd020000000p-2},{0x1.c7e8e60000000p-1,0x1.d2016e0000000p-2},{0x1.c89f580000000p-1,0x1.cf34ba0000000p-2},
{0x1.c954b20000000p-1,0x1.cc66ea0000000p-2},{0x1.ca08f20000000p-1,0x1.c997fc0000000p-2},{0x1.cabc160000000p-1,0x1.c6c7f40000000p-2},{0x1.cb6e200000000p-1,0x1.c3f6d40000000p-2},
{0x1.cc1f100000000p-1,0x1.c1249e0000000p-2},{0x1.cccee20000000p-1,0x1.be51520000000p-2},{0x1.cd7d980000000p-1,0x1.bb7cf20000000p-2},{0x1.ce2b320000000p-1,0x1.b8a7820000000p-2},
{0x1.ced7b00000000p-1,0x1.b5d1000000000p-2},{0x1.cf830e0000000p-1,0x1.b2f9720000000p-2},{0x1.d02d500000000p-1,0x1.b020d60000000p-2},{0x1.d0d6720000000p-1,0x1.ad47320000000p-2},
{0x1.d17e780000000p-1,0x1.aa6c820000000p-2},{0x1.d2255c0000000p-1,0x1.a790ce0000000p-2},{0x1.d2cb220000000p-1,0x1.a4b4120000000p-2},{0x1.d36fc80000000p-1,0x1.a1d6540000000p-2},
{0x1.d4134e0000000p-1,0x1.9ef7940000000p-2},{0x1.d4b5b20000000p-1,0x1.9c17d40000000p-2},{0x1.d556f60000000p-1,0x1.9937160000000p-2},{0x1.d5f7180000000p-1,0x1.96555c0000000p-2},
{0x1.d696180000000p-1,0x1.9372a60000000p-2},{0x1.d733f60000000p-1,0x1.908ef80000000p-2},{0x1.d7d0b00000000p-1,0x1.8daa520000000p-2},{0x1.d86c480000000p-1,0x1.8ac4b80000000p-2},
{0x1.d906bc0000000p-1,0x1.87de2a0000000p-2},{0x1.d9a00e0000000p-1,0x1.84f6aa0000000p-2},{0x1.da383a0000000p-1,0x1.820e3c0000000p-2},{0x1.dacf420000000p-1,0x1.7f24de0000000p-2},
{0x1.db65260000000p-1,0x1.7c3a940000000p-2},{0x1.dbf9e40000000p-1,0x1.794f5e0000000p-2},{0x1.dc8d7c0000000p-1,0x1.7663400000000p-2},{0x1.dd1ff00000000p-1,0x1.73763c0000000p-2},
{0x1.ddb13c0000000p-1,0x1.7088540000000p-2},{0x1.de41600000000p-1,0x1.6d99860000000p-2},{0x1.ded0600000000p-1,0x1.6aa9d80000000p-2},{0x1.df5e360000000p-1,0x1.67b94a0000000p-2},
{0x1.dfeae60000000p-1,0x1.64c7de0000000p-2},{0x1.e0766e0000000p-1,0x1.61d5960000000p-2},{0x1.e100cc0000000p-1,0x1.5ee2740000000p-2},{0x1.e18a020000000p-1,0x1.5bee780000000p-2},
{0x1.e212100000000p-1,0x1.58f9a80000000p-2},{0x1.e298f40000000p-1,0x1.5604020000000p-2},{0x1.e31eae0000000p-1,0x1.530d880000000p-2},{0x1.e3a33e0000000p-1,0x1.50163e0000000p-2},
{0x1.e426a40000000p-1,0x1.4d1e240000000p-2},{0x1.e4a8e00000000p-1,0x1.4a253e0000000p-2},{0x1.e529f00000000p-1,0x1.472b8a0000000p-2},{0x1.e5a9d60000000p-1,0x1.44310e0000000p-2},
{0x1.e6288e0000000p-1,0x1.4135ca0000000p-2},{0x1.e6a61c0000000p-1,0x1.3e39be0000000p-2},{0x1.e7227e0000000p-1,0x1.3b3cf00000000p-2},{0x1.e79db20000000p-1,0x1.383f5e0000000p-2},
{0x1.e817ba0000000p-1,0x1.35410c0000000p-2},{0x1.e890960000000p-1,0x1.3241fc0000000p-2},{0x1.e908440000000p-1,0x1.2f422e0000000p-2},{0x1.e97ec40000000p-1,0x1.2c41a40000000p-2},
{0x1.e9f4160000000p-1,0x1.2940620000000p-2},{0x1.ea683a0000000p-1,0x1.263e6a0000000p-2},{0x1.eadb2e0000000p-1,0x1.233bba0000000p-2},{0x1.eb4cf60000000p-1,0x1.2038580000000p-2},
{0x1.ebbd8c0000000p-1,0x1.1d34440000000p-2},{0x1.ec2cf40000000p-1,0x1.1a2f800000000p-2},{0x1.ec9b2e0000000p-1,0x1.172a0e0000000p-2},{0x1.ed08360000000p-1,0x1.1423ee0000000p-2},
{0x1.ed740e0000000p-1,0x1.111d260000000p-2},{0x1.eddeb60000000p-1,0x1.0e15b40000000p-2},{0x1.ee482e0000000p-1,0x1.0b0d9c0000000p-2},{0x1.eeb0740000000p-1,0x1.0804e00000000p-2},
{0x1.ef178a0000000p-1,0x1.04fb800000000p-2},{0x1.ef7d6e0000000p-1,0x1.01f1800000000p-2},{0x1.efe2200000000p-1,0x1.fdcdc20000000p-3},{0x1.f045a20000000p-1,0x1.f7b7480000000p-3},
{0x1.f0a7f00000000p-1,0x1.f19f980000000p-3},{0x1.f1090c0000000p-1,0x1.eb86b40000000p-3},{0x1.f168f60000000p-1,0x1.e56ca20000000p-3},{0x1.f1c7ac0000000p-1,0x1.df51640000000p-3},
{0x1.f225300000000p-1,0x1.d934fe0000000p-3},{0x1.f281800000000p-1,0x1.d317740000000p-3},{0x1.f2dc9c0000000p-1,0x1.ccf8cc0000000p-3},{0x1.f336860000000p-1,0x1.c6d9060000000p-3},
{0x1.f38f3a0000000p-1,0x1.c0b8260000000p-3},{0x1.f3e6bc0000000p-1,0x1.ba96340000000p-3},{0x1.f43d080000000p-1,0x1.b4732e0000000p-3},{0x1.f492200000000p-1,0x1.ae4f1e0000000p-3},
{0x1.f4e6040000000p-1,0x1.a82a020000000p-3},{0x1.f538b20000000p-1,0x1.a203e20000000p-3},{0x1.f58a2c0000000p-1,0x1.9bdcc00000000p-3},{0x1.f5da6e0000000p-1,0x1.95b49e0000000p-3},
{0x1.f6297c0000000p-1,0x1.8f8b840000000p-3},{0x1.f677560000000p-1,0x1.8961720000000p-3},{0x1.f6c3f80000000p-1,0x1.83366e0000000p-3},{0x1.f70f640000000p-1,0x1.7d0a7c0000000p-3},
{0x1.f7599a0000000p-1,0x1.76dd9e0000000p-3},{0x1.f7a29a0000000p-1,0x1.70afd80000000p-3},{0x1.f7ea620000000p-1,0x1.6a81300000000p-3},{0x1.f830f40000000p-1,0x1.6451a80000000p-3},
{0x1.f876500000000p-1,0x1.5e21440000000p-3},{0x1.f8ba740000000p-1,0x1.57f0080000000p-3},{0x1.f8fd600000000p-1,0x1.51bdf80000000p-3},{0x1.f93f140000000p-1,0x1.4b8b180000000p-3},
{0x1.f97f920000000p-1,0x1.45576c0000000p-3},{0x1.f9bed80000000p-1,0x1.3f22f60000000p-3},{0x1.f9fce60000000p-1,0x1.38edbc0000000p-3},{0x1.fa39ba0000000p-1,0x1.32b7c00000000p-3},
{0x1.fa75580000000p-1,0x1.2c81060000000p-3},{0x1.faafbc0000000p-1,0x1.2649940000000p-3},{0x1.fae8e80000000p-1,0x1.20116e0000000p-3},{0x1.fb20dc0000000p-1,0x1.19d8940000000p-3},
{0x1.fb57980000000p-1,0x1.139f0c0000000p-3},{0x1.fb8d180000000p-1,0x1.0d64dc0000000p-3},{0x1.fbc1620000000p-1,0x1.072a040000000p-3},{0x1.fbf4700000000p-1,0x1.00ee8a0000000p-3},
{0x1.fc26480000000p-1,0x1.f564e60000000p-4},{0x1.fc56e40000000p-1,0x1.e8eb800000000p-4},{0x1.fc86460000000p-1,0x1.dc70ec0000000p-4},{0x1.fcb4700000000p-1,0x1.cff5340000000p-4},
{0x1.fce1600000000p-1,0x1.c3785c0000000p-4},{0x1.fd0d160000000p-1,0x1.b6fa6e0000000p-4},{0x1.fd37920000000p-1,0x1.aa7b720000000p-4},{0x1.fd60d20000000p-1,0x1.9dfb6e0000000p-4},
{0x1.fd88da0000000p-1,0x1.917a6c0000000p-4},{0x1.fdafa80000000p-1,0x1.84f8720000000p-4},{0x1.fdd53a0000000p-1,0x1.7875860000000p-4},{0x1.fdf9920000000p-1,0x1.6bf1b40000000p-4},
{0x1.fe1cb00000000p-1,0x1.5f6d000000000p-4},{0x1.fe3e920000000p-1,0x1.52e7740000000p-4},{0x1.fe5f3a0000000p-1,0x1.4661180000000p-4},{0x1.fe7ea80000000p-1,0x1.39d9f20000000p-4},
{0x1.fe9cda0000000p-1,0x1.2d520a0000000p-4},{0x1.feb9d20000000p-1,0x1.20c9680000000p-4},{0x1.fed58e0000000p-1,0x1.1440140000000p-4},{0x1.fef0100000000p-1,0x1.07b6140000000p-4},
{0x1.ff09560000000p-1,0x1.f656e80000000p-5},{0x1.ff21620000000p-1,0x1.dd40700000000p-5},{0x1.ff38300000000p-1,0x1.c428d20000000p-5},{0x1.ff4dc60000000p-1,0x1.ab101c0000000p-5},
{0x1.ff621e0000000p-1,0x1.91f6600000000p-5},{0x1.ff753c0000000p-1,0x1.78dbaa0000000p-5},{0x1.ff871e0000000p-1,0x1.5fc00e0000000p-5},{0x1.ff97c40000000p-1,0x1.46a3960000000p-5},
{0x1.ffa72e0000000p-1,0x1.2d86580000000p-5},{0x1.ffb55e0000000p-1,0x1.14685e0000000p-5},{0x1.ffc2520000000p-1,0x1.f693740000000p-6},{0x1.ffce0a0000000p-1,0x1.c454f40000000p-6},
{0x1.ffd8860000000p-1,0x1.9215600000000p-6},{0x1.ffe1c60000000p-1,0x1.5fd4d20000000p-6},{0x1.ffe9cc0000000p-1,0x1.2d936c0000000p-6},{0x1.fff0940000000p-1,0x1.f6a2960000000p-7},
{0x1.fff6220000000p-1,0x1.921d200000000p-7},{0x1.fffa720000000p-1,0x1.2d96b00000000p-7},{0x1.fffd880000000p-1,0x1.921f100000000p-8},{0x1.ffff620000000p-1,0x1.921f8c0000000p-9},
};
typedef unsigned short u16;
typedef __attribute__((ext_vector_type(8))) short bf16x8;
typedef __attribute__((ext_vector_type(4))) float f32x4;

constexpr int S_ = 2048, D_ = 640, DF_ = 321, F_ = 1728;

__device__ __forceinline__ float bf2f(u16 v) { return __uint_as_float(((unsigned)v) << 16); }
__device__ __forceinline__ u16 f2bf(float f) {
  __hip_bfloat16 b = __float2bfloat16(f);
  return *reinterpret_cast<u16*>(&b);
}
__device__ __forceinline__ float rbf(float f) { return bf2f(f2bf(f)); }
__device__ __forceinline__ float2 cmul(float2 a, float2 b) { return make_float2(a.x * b.x - a.y * b.y, a.x * b.y + a.y * b.x); }
__device__ __forceinline__ float2 cmulc(float2 a, float2 b) { return make_float2(a.x * b.x + a.y * b.y, a.y * b.x - a.x * b.y); }
__device__ __forceinline__ float2 cadd(float2 a, float2 b) { return make_float2(a.x + b.x, a.y + b.y); }
__device__ __forceinline__ float2 csub(float2 a, float2 b) { return make_float2(a.x - b.x, a.y - b.y); }
template <int CTRL> __device__ __forceinline__ float dppf(float v) {
  return __int_as_float(__builtin_amdgcn_mov_dpp(__float_as_int(v), CTRL, 0xf, 0xf, false));
}
template <int H> __device__ __forceinline__ float xl(float v) {
  if (H == 1) return dppf<0xB1>(v);
  if (H == 2) return dppf<0x4E>(v);
  if (H == 4) return dppf<0x1B>(dppf<0x141>(v));
  if (H == 8) return dppf<0x128>(v);
  if (H == 16) {
    auto r = __builtin_amdgcn_permlane16_swap(__float_as_uint(v), __float_as_uint(v), false, false);
    return __uint_as_float((threadIdx.x & 16) ? r[0] : r[1]);
  }
  auto r = __builtin_amdgcn_permlane32_swap(__float_as_uint(v), __float_as_uint(v), false, false);
  return __uint_as_float((threadIdx.x & 32) ? r[0] : r[1]);
}
__device__ __forceinline__ float wave_sum(float v) {
  v += xl<1>(v); v += xl<2>(v); v += xl<4>(v); v += xl<8>(v); v += xl<16>(v); v += xl<32>(v);
  return v;
}
template <int H> __device__ __forceinline__ float2 shx(float2 v) { return make_float2(xl<H>(v.x), xl<H>(v.y)); }
template <int H> __device__ __forceinline__ void stage_fwd(float2* z, float2 tw, int lane) {
  const float sg = (lane & H) ? -1.f : 1.f;
#pragma unroll
  for (int b = 0; b < 5; ++b) {
    float2 p = shx<H>(z[b]);
    float2 t = make_float2(__builtin_fmaf(sg, z[b].x, p.x), __builtin_fmaf(sg, z[b].y, p.y));
    z[b] = cmul(t, tw);
  }
}
template <int H> __device__ __forceinline__ void stage_inv(float2* z, float2 tw, int lane) {
  const float sg = (lane & H) ? -1.f : 1.f;
#pragma unroll
  for (int b = 0; b < 5; ++b) {
    float2 a = cmulc(z[b], tw);
    float2 p = shx<H>(a);
    z[b] = make_float2(__builtin_fmaf(sg, a.x, p.x), __builtin_fmaf(sg, a.y, p.y));
  }
}
template <int SGN>
__device__ __forceinline__ void dft5(float2* a) {
  const float c1 = 0.30901699437494745f, c2 = -0.8090169943749475f;
  const float s1 = 0.9510565162951535f, s2 = 0.5877852522924731f;
  float2 t1 = cadd(a[1], a[4]), t2 = cadd(a[2], a[3]), t3 = csub(a[1], a[4]), t4 = csub(a[2], a[3]);
  float2 m1 = make_float2(a[0].x + c1 * t1.x + c2 * t2.x, a[0].y + c1 * t1.y + c2 * t2.y);
  float2 m2 = make_float2(a[0].x + c2 * t1.x + c1 * t2.x, a[0].y + c2 * t1.y + c1 * t2.y);
  float2 n1 = make_float2(s1 * t3.x + s2 * t4.x, s1 * t3.y + s2 * t4.y);
  float2 n2 = make_float2(s2 * t3.x - s1 * t4.x, s2 * t3.y - s1 * t4.y);
  a[0] = make_float2(a[0].x + t1.x + t2.x, a[0].y + t1.y + t2.y);
  float2 in1 = make_float2(-n1.y, n1.x), in2 = make_float2(-n2.y, n2.x);
  if (SGN < 0) {
    a[1] = csub(m1, in1); a[4] = cadd(m1, in1); a[2] = csub(m2, in2); a[3] = cadd(m2, in2);
  } else {
    a[1] = cadd(m1, in1); a[4] = csub(m1, in1); a[2] = cadd(m2, in2); a[3] = csub(m2, in2);
  }
}
template <int SGN>
__device__ __forceinline__ void dft4(float2& a, float2& b, float2& c, float2& d) {
  float2 t0 = cadd(a, c), t1 = csub(a, c), t2 = cadd(b, d), t3 = csub(b, d);
  float2 r = (SGN < 0) ? make_float2(t3.y, -t3.x) : make_float2(-t3.y, t3.x);
  a = cadd(t0, t2); c = csub(t0, t2); b = cadd(t1, r); d = csub(t1, r);
}
template <int SGN>
__device__ __forceinline__ void dft8(float2* v) {
  const float h = 0.70710678118654752f;
  float2 e0 = v[0], e1 = v[2], e2 = v[4], e3 = v[6];
  float2 o0 = v[1], o1 = v[3], o2 = v[5], o3 = v[7];
  dft4<SGN>(e0, e1, e2, e3);
  dft4<SGN>(o0, o1, o2, o3);
  if (SGN < 0) {
    o1 = make_float2(h * (o1.x + o1.y), h * (o1.y - o1.x));
    o2 = make_float2(o2.y, -o2.x);
    o3 = make_float2(h * (o3.y - o3.x), -h * (o3.x + o3.y));
  } else {
    o1 = make_float2(h * (o1.x - o1.y), h * (o1.y + o1.x));
    o2 = make_float2(-o2.y, o2.x);
    o3 = make_float2(-h * (o3.x + o3.y), h * (o3.x - o3.y));
  }
  v[0] = cadd(e0, o0); v[4] = csub(e0, o0);
  v[1] = cadd(e1, o1); v[5] = csub(e1, o1);
  v[2] = cadd(e2, o2); v[6] = csub(e2, o2);
  v[3] = cadd(e3, o3); v[7] = csub(e3, o3);
}

__device__ __forceinline__ void ln_row(float* xe, float* xo) {
  float s = 0.f;
#pragma unroll
  for (int a = 0; a < 5; ++a) s += xe[a] + xo[a];
  s = wave_sum(s);
  float mean = s * (1.0f / 640.0f);
  float q = 0.f;
#pragma unroll
  for (int a = 0; a < 5; ++a) { float d0 = xe[a] - mean, d1 = xo[a] - mean; q += d0 * d0 + d1 * d1; }
  q = wave_sum(q);
  float var = rbf(q * (1.0f / 640.0f));
  float mb = rbf(mean);
  float sd = rbf(sqrtf(rbf(var + 1e-5f)));
#pragma unroll
  for (int a = 0; a < 5; ++a) { xe[a] = rbf(rbf(xe[a] - mb) / sd); xo[a] = rbf(rbf(xo[a] - mb) / sd); }
}

constexpr int ZS = 324;

struct PrepArgs {
  const u16* wr; const u16* wi; float2* Wt;
  const u16* w1; const u16* w3; const u16* w2; u16* w13t; u16* w2t;
};
__device__ __forceinline__ void prep_block(const PrepArgs& a, int b, char* smem) {
  const int t = threadIdx.x;
  if (b < 192) {
    float2(*tl)[65] = (float2(*)[65])smem;
    const int p0 = (b & 31) * 64, k0 = (b >> 5) * 64;
    const float sc = 1.0f / (2048.0f * 640.0f);
#pragma unroll
    for (int e = t; e < 4096; e += 512) {
      int i = e >> 6, j = e & 63, p = p0 + i, k = k0 + j;
      int f = (p >> 8) + 8 * ((p >> 5) & 7) + 64 * ((p >> 2) & 7) + 512 * (p & 3);
      if (k < DF_) tl[j][i] = make_float2(bf2f(a.wr[(size_t)f * DF_ + k]) * sc, bf2f(a.wi[(size_t)f * DF_ + k]) * sc);
    }
    __syncthreads();
#pragma unroll
    for (int e = t; e < 4096; e += 512) {
      int j = e >> 6, i = e & 63, k = k0 + j;
      if (k < DF_) a.Wt[(size_t)k * S_ + p0 + i] = tl[j][i];
    }
  } else {
    u16* ts = (u16*)smem;
    int bb = b - 192;
    const u16* src; u16* dst; int sld, dld, kt, ct, which = 0; bool is13 = bb < 540;
    if (is13) { which = bb / 270; int r = bb % 270; kt = r / 27; ct = r % 27; src = which ? a.w3 : a.w1; sld = F_; dst = a.w13t; dld = D_; }
    else { int r = bb - 540; kt = r / 10; ct = r % 10; src = a.w2; sld = D_; dst = a.w2t; dld = F_; }
    {
      int i = t >> 3, c = t & 7;
      uint4 v = *(const uint4*)(src + (size_t)(kt * 64 + i) * sld + ct * 64 + c * 8);
      *(uint4*)(ts + i * 72 + c * 8) = v;
    }
    __syncthreads();
    {
      int j = t >> 3, c = t & 7;
      u16 v[8];
#pragma unroll
      for (int q = 0; q < 8; ++q) v[q] = ts[(c * 8 + q) * 72 + j];
      int f = ct * 64 + j;
      int n = is13 ? ((f >> 4) * 32 + which * 16 + (f & 15)) : f;
      uint4 o;
      o.x = v[0] | ((unsigned)v[1] << 16); o.y = v[2] | ((unsigned)v[3] << 16);
      o.z = v[4] | ((unsigned)v[5] << 16); o.w = v[6] | ((unsigned)v[7] << 16);
      *(uint4*)(dst + (size_t)n * dld + kt * 64 + c * 8) = o;
    }
  }
}

__global__ __launch_bounds__(512) void k_rows_fwd(const u16* __restrict__ x, float2* __restrict__ Xt, PrepArgs pa) {
  __shared__ __attribute__((aligned(16))) char smem[64 * 65 * 8];
  if (blockIdx.x >= 256) { prep_block(pa, blockIdx.x - 256, smem); return; }
  float2* zs = (float2*)smem;
  const int wave = threadIdx.x >> 6, lane = threadIdx.x & 63;
  const int row = blockIdx.x * 8 + wave;
  const unsigned* xr = (const unsigned*)(x + (size_t)row * D_);
  unsigned xv[5];
#pragma unroll
  for (int a = 0; a < 5; ++a) xv[a] = xr[lane + 64 * a];
  float2 tb[4], th[6], tp[6];
#pragma unroll
  for (int b = 1; b < 5; ++b) tb[b - 1] = TW640[2 * lane * b];
#pragma unroll
  for (int s = 0; s < 6; ++s) { int h = 32 >> s; th[s] = TW640[(lane & h) ? (lane & (h - 1)) * (320 / h) : 0]; }
  const int r = threadIdx.x & 7, kk = threadIdx.x >> 3;
#pragma unroll
  for (int j = 0; j < 5; ++j) tp[j] = TW640[kk + 64 * j];
  tp[5] = make_float2(-1.f, 0.f);
  float xe[5], xo[5];
#pragma unroll
  for (int a = 0; a < 5; ++a) { xe[a] = bf2f(xv[a] & 0xffff); xo[a] = bf2f(xv[a] >> 16); }
  ln_row(xe, xo);
  float2 z[5];
#pragma unroll
  for (int a = 0; a < 5; ++a) z[a] = make_float2(xe[a], xo[a]);
  dft5<-1>(z);
#pragma unroll
  for (int b = 1; b < 5; ++b) z[b] = cmul(z[b], tb[b - 1]);
  stage_fwd<32>(z, th[0], lane); stage_fwd<16>(z, th[1], lane); stage_fwd<8>(z, th[2], lane);
  stage_fwd<4>(z, th[3], lane); stage_fwd<2>(z, th[4], lane); stage_fwd<1>(z, th[5], lane);
  const int m = __brev((unsigned)lane) >> 26;
#pragma unroll
  for (int b = 0; b < 5; ++b) zs[wave * ZS + 5 * m + b] = z[b];
  __syncthreads();
#pragma unroll
  for (int j = 0; j < 6; ++j) {
    int k = kk + 64 * j;
    if (k <= 320) {
      float2 A = zs[r * ZS + (k == 320 ? 0 : k)], B = zs[r * ZS + (k == 0 ? 0 : 320 - k)];
      B.y = -B.y;
      float2 e = make_float2(0.5f * (A.x + B.x), 0.5f * (A.y + B.y));
      float2 o = make_float2(0.5f * (A.x - B.x), 0.5f * (A.y - B.y));
      float2 t = cmul(o, tp[j]);
      Xt[(size_t)k * S_ + blockIdx.x * 8 + r] = make_float2(e.x + t.y, e.y - t.x);
    }
  }
}

__device__ __forceinline__ int PL(int i) { return i + 4 * (i >> 5); }
__device__ __forceinline__ void tw7(float2 w1, float2 w2, float2 w4, float2* o) {
  o[0] = w1; o[1] = w2; o[2] = cmul(w1, w2); o[3] = w4; o[4] = cmul(w1, w4); o[5] = cmul(w2, w4); o[6] = cmul(o[2], w4);
}
__global__ __launch_bounds__(256) void k_cols(float2* __restrict__ Xt, const float2* __restrict__ Wt) {
  __shared__ float2 ls[2][2304];
  const int t = threadIdx.x;
  float2* col = Xt + (size_t)blockIdx.x * S_;
  float2 v[8];
#pragma unroll
  for (int j = 0; j < 8; ++j) v[j] = col[t + 256 * j];
  float2 a1 = TW2048[t], a2 = TW2048[2 * t], a4 = TW2048[4 * t];
  float2 b1 = TW2048[8 * (t & 31)], b2 = TW2048[16 * (t & 31)], b4 = TW2048[32 * (t & 31)];
  float2 c1 = TW2048[64 * (t & 3)], c2 = TW2048[128 * (t & 3)], c4 = TW2048[256 * (t & 3)];
  float4 wq[4];
  {
    const float4* wp = (const float4*)(Wt + (size_t)blockIdx.x * S_ + 8 * t);
#pragma unroll
    for (int e = 0; e < 4; ++e) wq[e] = wp[e];
  }
  float2 tw[7];
  dft8<-1>(v);
  tw7(a1, a2, a4, tw);
#pragma unroll
  for (int k = 1; k < 8; ++k) v[k] = cmul(v[k], tw[k - 1]);
#pragma unroll
  for (int k = 0; k < 8; ++k) ls[0][PL(k * 256 + t)] = v[k];
  __syncthreads();
  {
    int k = t >> 5, u = t & 31;
#pragma unroll
    for (int j = 0; j < 8; ++j) v[j] = ls[0][PL(k * 256 + u + 32 * j)];
    dft8<-1>(v);
    tw7(b1, b2, b4, tw);
#pragma unroll
    for (int k2 = 1; k2 < 8; ++k2) v[k2] = cmul(v[k2], tw[k2 - 1]);
#pragma unroll
    for (int k2 = 0; k2 < 8; ++k2) ls[1][PL((k * 8 + k2) * 32 + u)] = v[k2];
  }
  __syncthreads();
  {
    int kk = t >> 2, u = t & 3;
#pragma unroll
    for (int j = 0; j < 8; ++j) v[j] = ls[1][PL(kk * 32 + u + 4 * j)];
    dft8<-1>(v);
    tw7(c1, c2, c4, tw);
#pragma unroll
    for (int k3 = 1; k3 < 8; ++k3) v[k3] = cmul(v[k3], tw[k3 - 1]);
#pragma unroll
    for (int k3 = 0; k3 < 8; ++k3) ls[0][PL((kk * 8 + k3) * 4 + u)] = v[k3];
  }
  __syncthreads();
  {
#pragma unroll
    for (int e = 0; e < 8; ++e) v[e] = ls[0][PL(8 * t + e)];
    dft4<-1>(v[0], v[1], v[2], v[3]);
    dft4<-1>(v[4], v[5], v[6], v[7]);
#pragma unroll
    for (int e = 0; e < 4; ++e) {
      float4 w = wq[e];
      v[2 * e] = cmul(v[2 * e], make_float2(w.x, w.y));
      v[2 * e + 1] = cmul(v[2 * e + 1], make_float2(w.z, w.w));
    }
    dft4<1>(v[0], v[1], v[2], v[3]);
    dft4<1>(v[4], v[5], v[6], v[7]);
#pragma unroll
    for (int e = 0; e < 8; ++e) ls[1][PL(8 * t + e)] = v[e];
  }
  __syncthreads();
  {
    int kk = t >> 2, u = t & 3;
#pragma unroll
    for (int k3 = 0; k3 < 8; ++k3) v[k3] = ls[1][PL((kk * 8 + k3) * 4 + u)];
#pragma unroll
    for (int k3 = 1; k3 < 8; ++k3) v[k3] = cmulc(v[k3], tw[k3 - 1]);
    dft8<1>(v);
#pragma unroll
    for (int j = 0; j < 8; ++j) ls[0][PL(kk * 32 + u + 4 * j)] = v[j];
  }
  __syncthreads();
  {
    int k = t >> 5, u = t & 31;
#pragma unroll
    for (int k2 = 0; k2 < 8; ++k2) v[k2] = ls[0][PL((k * 8 + k2) * 32 + u)];
    tw7(b1, b2, b4, tw);
#pragma unroll
    for (int k2 = 1; k2 < 8; ++k2) v[k2] = cmulc(v[k2], tw[k2 - 1]);
    dft8<1>(v);
#pragma unroll
    for (int j = 0; j < 8; ++j) ls[1][PL(k * 256 + u + 32 * j)] = v[j];
  }
  __syncthreads();
  {
#pragma unroll
    for (int k = 0; k < 8; ++k) v[k] = ls[1][PL(k * 256 + t)];
    tw7(a1, a2, a4, tw);
#pragma unroll
    for (int k = 1; k < 8; ++k) v[k] = cmulc(v[k], tw[k - 1]);
    dft8<1>(v);
#pragma unroll
    for (int j = 0; j < 8; ++j) col[t + 256 * j] = v[j];
  }
}

__global__ __launch_bounds__(512) void k_rows_inv(const float2* __restrict__ Zt, const u16* __restrict__ xin,
                                                   u16* __restrict__ xout, u16* __restrict__ xn) {
  __shared__ float2 ys[8 * ZS];
  const int wave = threadIdx.x >> 6, lane = threadIdx.x & 63;
  const int row = blockIdx.x * 8 + wave;
  const int m = __brev((unsigned)lane) >> 26;
  {
    const int r = threadIdx.x & 7, kk = threadIdx.x >> 3;
    float2 y[6];
#pragma unroll
    for (int j = 0; j < 6; ++j) {
      int k = kk + 64 * j;
      if (k <= 320) y[j] = Zt[(size_t)k * S_ + blockIdx.x * 8 + r];
    }
#pragma unroll
    for (int j = 0; j < 6; ++j) {
      int k = kk + 64 * j;
      if (k <= 320) {
        if (k == 0 || k == 320) y[j].y = 0.f;
        ys[r * ZS + k] = y[j];
      }
    }
  }
  const unsigned* xr = (const unsigned*)(xin + (size_t)row * D_);
  unsigned xv[5];
#pragma unroll
  for (int a = 0; a < 5; ++a) xv[a] = xr[lane + 64 * a];
  float2 tb[4], th[6], tq[5];
#pragma unroll
  for (int b = 1; b < 5; ++b) tb[b - 1] = TW640[2 * lane * b];
#pragma unroll
  for (int s = 0; s < 6; ++s) { int h = 1 << s; th[s] = TW640[(lane & h) ? (lane & (h - 1)) * (320 / h) : 0]; }
#pragma unroll
  for (int b = 0; b < 5; ++b) tq[b] = TW640[5 * m + b];
  __syncthreads();
  float2 z[5];
#pragma unroll
  for (int b = 0; b < 5; ++b) {
    int k = 5 * m + b;
    float2 A = ys[wave * ZS + k], B = ys[wave * ZS + 320 - k];
    B.y = -B.y;
    float2 e = cadd(A, B), o = csub(A, B);
    float2 t = cmulc(o, tq[b]);
    z[b] = make_float2(e.x - t.y, e.y + t.x);
  }
  stage_inv<1>(z, th[0], lane); stage_inv<2>(z, th[1], lane); stage_inv<4>(z, th[2], lane);
  stage_inv<8>(z, th[3], lane); stage_inv<16>(z, th[4], lane); stage_inv<32>(z, th[5], lane);
#pragma unroll
  for (int b = 1; b < 5; ++b) z[b] = cmulc(z[b], tb[b - 1]);
  dft5<1>(z);
  unsigned* xo_ = (unsigned*)(xout + (size_t)row * D_);
  unsigned* xn_ = (unsigned*)(xn + (size_t)row * D_);
  float xe[5], xo[5];
#pragma unroll
  for (int a = 0; a < 5; ++a) {
    xe[a] = rbf(bf2f(xv[a] & 0xffff) + rbf(z[a].x));
    xo[a] = rbf(bf2f(xv[a] >> 16) + rbf(z[a].y));
    xo_[lane + 64 * a] = (unsigned)f2bf(xe[a]) | ((unsigned)f2bf(xo[a]) << 16);
  }
  ln_row(xe, xo);
#pragma unroll
  for (int a = 0; a < 5; ++a) xn_[lane + 64 * a] = (unsigned)f2bf(xe[a]) | ((unsigned)f2bf(xo[a]) << 16);
}

template <int N> __device__ __forceinline__ void wait_c() { asm volatile("s_waitcnt vmcnt(%0)" ::"i"(N) : "memory"); }
template <int L> __device__ __forceinline__ void wait_dyn(int a) {
  if (a <= 0) wait_c<0>(); else if (a == 1) wait_c<L>(); else if (a == 2) wait_c<2 * L>(); else if (a == 3) wait_c<3 * L>();
  else if (a == 4) wait_c<4 * L>(); else if (a == 5) wait_c<5 * L>(); else wait_c<6 * L>();
}
__device__ __forceinline__ unsigned pk2(float lo, float hi) { return (unsigned)f2bf(lo) | ((unsigned)f2bf(hi) << 16); }
template <int BM, int BN, int WM, int WN, int WK, int NS, int EPI, int K, int N, int MODE = 0, int DS = 0>
__global__ __launch_bounds__(WM * WN * WK * 64) void k_gemm(const u16* __restrict__ A, const u16* __restrict__ Bt,
                                                            u16* __restrict__ out, const u16* __restrict__ res) {
  constexpr int NW = WM * WN * WK, NT = NW * 64, BK = 64, KS = 2 / WK;
  constexpr int TM = BM / WM / 16, TN = BN / WN / 16;
  constexpr int STG = (BM + BN) * BK * 2;
  constexpr int NI = (BM + BN) / 8;
  constexpr int LMAX = (NI + NW - 1) / NW;
  static_assert(LMAX * (NS - 1) < 64 && NS <= 8, "vmcnt range");
  __shared__ __attribute__((aligned(16))) char smem[NS * STG];
  const int tid = threadIdx.x, lane = tid & 63, fr = lane & 15, fq = lane >> 4;
  const int wid = __builtin_amdgcn_readfirstlane(tid >> 6);
  const int wk = wid % WK, wn = (wid / WK) % WN, wm = wid / (WK * WN);
  constexpr int nbm = 2048 / BM, nbn = N / BN, nwg = nbm * nbn;
  int id = blockIdx.x;
  { int q = nwg / 8, r = nwg % 8, xcd = id % 8, off = id / 8; id = (xcd < r ? xcd * (q + 1) : r * (q + 1) + (xcd - r) * q) + off; }
  const int bm = (EPI == 0) ? id % nbm : id / nbn, bn = (EPI == 0) ? id / nbm : id % nbn;
  const int Lw = (NI - wid + NW - 1) / NW;
  const u16* gsrc[(NI + NW - 1) / NW];
#pragma unroll
  for (int i = 0; i < (NI + NW - 1) / NW; ++i) {
    int ins = i * NW + wid; if (ins >= NI) ins = NI - 1;
    int c = ins * 64 + lane, r = c >> 3, cc = (c & 7) ^ (r & 7);
    gsrc[i] = ((r < BM) ? (A + (size_t)(bm * BM + r) * K) : (Bt + (size_t)(bn * BN + r - BM) * K)) + cc * 8;
  }
  auto stage = [&](int buf, int kt) {
    char* sbase = smem + buf * STG;
#pragma unroll
    for (int i = 0; i < (NI + NW - 1) / NW; ++i) {
      int ins = i * NW + wid;
      if ((i * NW + NW <= NI || ins < NI) && MODE != 1)
        __builtin_amdgcn_global_load_lds((const unsigned*)(gsrc[i] + kt * BK), (unsigned*)(sbase + ins * 1024), 16, 0, 0);
    }
  };
  f32x4 acc[TM][TN];
#pragma unroll
  for (int i = 0; i < TM; ++i)
#pragma unroll
    for (int j = 0; j < TN; ++j) acc[i][j] = f32x4{0.f, 0.f, 0.f, 0.f};
  int aoff[KS][TM], boff[KS][TN];
#pragma unroll
  for (int q = 0; q < KS; ++q) {
    int kk = (WK == 2) ? wk : q;
#pragma unroll
    for (int i = 0; i < TM; ++i) { int r = wm * (BM / WM) + i * 16 + fr; aoff[q][i] = r * 128 + (((kk * 4 + fq) ^ (r & 7)) << 4); }
#pragma unroll
    for (int j = 0; j < TN; ++j) { int r = wn * (BN / WN) + j * 16 + fr; boff[q][j] = BM * 128 + r * 128 + (((kk * 4 + fq) ^ (r & 7)) << 4); }
  }
  constexpr int nt = K / BK;
#pragma unroll
  for (int s = 0; s < NS; ++s) if (s < nt) stage(s, s);
  bf16x8 af[2][KS][TM], bfr[2][KS][TN];
  auto readf = [&](int set, int buf) {
    const char* sa = smem + buf * STG;
#pragma unroll
    for (int q = 0; q < KS; ++q) {
#pragma unroll
      for (int i = 0; i < TM; ++i) af[set][q][i] = *(const bf16x8*)(sa + aoff[q][i]);
#pragma unroll
      for (int j = 0; j < TN; ++j) bfr[set][q][j] = *(const bf16x8*)(sa + boff[q][j]);
    }
  };
  auto mma = [&](int set) {
#pragma unroll
    for (int q = 0; q < KS; ++q)
#pragma unroll
      for (int i = 0; i < TM; ++i)
#pragma unroll
        for (int j = 0; j < TN; ++j) {
          if (MODE != 2) acc[i][j] = __builtin_amdgcn_mfma_f32_16x16x32_bf16(bfr[set][q][j], af[set][q][i], acc[i][j], 0, 0, 0);
          else asm volatile("" ::"v"(af[set][q][i]), "v"(bfr[set][q][j]));
        }
  };
  const bool full = (Lw == LMAX);
  if (full) wait_c<LMAX * ((nt < NS ? nt : NS) - 1)>(); else wait_c<(LMAX - 1) * ((nt < NS ? nt : NS) - 1)>();
  __builtin_amdgcn_s_barrier();
  readf(0, 0);
#pragma unroll
  for (int k0 = 0; k0 < nt; ++k0) {
    if (k0 + 1 < nt) {
      constexpr int dummy = 0; (void)dummy;
      const int issued = (NS + k0 > nt) ? nt : NS + k0;
      const int ahead = issued - (k0 + 2);
      if (NI % NW == 0) { wait_dyn<LMAX>(ahead); }
      else { if (full) wait_dyn<LMAX>(ahead); else wait_dyn<LMAX - 1>(ahead); }
      asm volatile("s_waitcnt lgkmcnt(0)" ::: "memory");
      __builtin_amdgcn_s_barrier();
      if (k0 + NS < nt) stage(k0 % NS, k0 + NS);
      readf((k0 + 1) & 1, (k0 + 1) % NS);
    }
    if (DS == 2) __builtin_amdgcn_s_setprio(1);
    mma(k0 & 1);
    if (DS == 2) __builtin_amdgcn_s_setprio(0);
  }
  if (EPI == 0 && DS == 1 && WK == 1) {
    const int NH = N / 2;
#pragma unroll
    for (int i = 0; i < TM; ++i)
#pragma unroll
      for (int j = 0; j < TN; j += 2) {
        float hv[4];
#pragma unroll
        for (int e = 0; e < 4; ++e) {
          float g = rbf(acc[i][j][e]), u = rbf(acc[i][j + 1][e]);
          float sl = rbf(g * __builtin_amdgcn_rcpf(1.0f + __expf(-g)));
          hv[e] = sl * u;
        }
        int row = bm * BM + wm * (BM / WM) + i * 16 + fr;
        int col = (bn * BN + wn * (BN / WN) + j * 16) / 2 + fq * 4;
        *(uint2*)(out + (size_t)row * NH + col) = make_uint2(pk2(hv[0], hv[1]), pk2(hv[2], hv[3]));
      }
    return;
  }
  __syncthreads();
  if (WK == 2) {
    f32x4* sr = (f32x4*)smem;
    if (wk == 1) {
#pragma unroll
      for (int i = 0; i < TM; ++i)
#pragma unroll
        for (int j = 0; j < TN; ++j) sr[((wid >> 1) * TM * TN + i * TN + j) * 64 + lane] = acc[i][j];
    }
    __syncthreads();
    if (wk == 0) {
#pragma unroll
      for (int i = 0; i < TM; ++i)
#pragma unroll
        for (int j = 0; j < TN; ++j) acc[i][j] += sr[((wid >> 1) * TM * TN + i * TN + j) * 64 + lane];
    }
    __syncthreads();
  }
  if (EPI == 0) {
    constexpr int OW = BN / 2, LD = OW + 8;
    u16* so = (u16*)smem;
    if (wk == 0) {
#pragma unroll
      for (int i = 0; i < TM; ++i)
#pragma unroll
        for (int j = 0; j < TN; j += 2) {
          float hv[4];
#pragma unroll
          for (int e = 0; e < 4; ++e) {
            float g = rbf(acc[i][j][e]), u = rbf(acc[i][j + 1][e]);
            float sl = rbf(g * __builtin_amdgcn_rcpf(1.0f + __expf(-g)));
            hv[e] = sl * u;
          }
          int row = wm * (BM / WM) + i * 16 + fr;
          int col = (wn * (BN / WN) + j * 16) / 2 + fq * 4;
          *(uint2*)(so + row * LD + col) = make_uint2(pk2(hv[0], hv[1]), pk2(hv[2], hv[3]));
        }
    }
    __syncthreads();
    const int NH = N / 2;
    constexpr int CPR = OW / 8;
    for (int c = tid; c < BM * CPR; c += NT) {
      int row = c / CPR, q = c % CPR;
      *(uint4*)(out + (size_t)(bm * BM + row) * NH + bn * OW + q * 8) = *(const uint4*)(so + row * LD + q * 8);
    }
  } else {
    constexpr int LD = BN + 8;
    u16* so = (u16*)smem;
    if (wk == 0) {
#pragma unroll
      for (int i = 0; i < TM; ++i)
#pragma unroll
        for (int j = 0; j < TN; ++j) {
          int row = wm * (BM / WM) + i * 16 + fr;
          int col = wn * (BN / WN) + j * 16 + fq * 4;
          *(uint2*)(so + row * LD + col) = make_uint2(pk2(acc[i][j][0], acc[i][j][1]), pk2(acc[i][j][2], acc[i][j][3]));
        }
    }
    __syncthreads();
    constexpr int CPR = BN / 8;
    for (int c = tid; c < BM * CPR; c += NT) {
      int row = c / CPR, q = c % CPR;
      size_t o = (size_t)(bm * BM + row) * N + bn * BN + q * 8;
      uint4 rv = *(const uint4*)(res + o), ov = *(const uint4*)(so + row * LD + q * 8), w;
      unsigned* rp = (unsigned*)&rv; unsigned* op = (unsigned*)&ov; unsigned* wp = (unsigned*)&w;
#pragma unroll
      for (int q2 = 0; q2 < 4; ++q2) {
        float lo = bf2f(rp[q2] & 0xffff) + bf2f(op[q2] & 0xffff), hi = bf2f(rp[q2] >> 16) + bf2f(op[q2] >> 16);
        wp[q2] = pk2(lo, hi);
      }
      *(uint4*)(out + o) = w;
    }
  }
}
extern "C" void kernel_launch(void* const* d_in, const int* in_sizes, int n_in, void* d_out, int out_size, void* d_ws,
                              size_t ws_size, hipStream_t stream) {
  const u16* x0 = (const u16*)d_in[0];
  char* ws = (char*)d_ws;
  size_t off = 0;
  auto take = [&](size_t bytes) { char* p = ws + off; off += (bytes + 255) & ~(size_t)255; return p; };
  float2* Xt = (float2*)take((size_t)DF_ * S_ * 8);
  float2* Wt = (float2*)take((size_t)DF_ * S_ * 8);
  u16* w13t = (u16*)take((size_t)2 * F_ * D_ * 2);
  u16* w2t = (u16*)take((size_t)F_ * D_ * 2);
  u16* xa = (u16*)take((size_t)S_ * D_ * 2);
  u16* xb = (u16*)take((size_t)S_ * D_ * 2);
  u16* xn = (u16*)take((size_t)S_ * D_ * 2);
  u16* h = (u16*)take((size_t)S_ * F_ * 2);
  u16* out = (u16*)d_out;
  PrepArgs pa;
  pa.wr = (const u16*)d_in[1]; pa.wi = (const u16*)d_in[2]; pa.Wt = Wt;
  pa.w1 = (const u16*)d_in[4]; pa.w3 = (const u16*)d_in[5]; pa.w2 = (const u16*)d_in[6]; pa.w13t = w13t; pa.w2t = w2t;

  const u16* xin = x0;
  for (int l = 0; l < 2; ++l) {
    k_rows_fwd<<<l == 0 ? 256 + 192 + 540 + 270 : 256, 512, 0, stream>>>(xin, Xt, pa);
    k_cols<<<DF_, 256, 0, stream>>>(Xt, Wt);
    k_rows_inv<<<256, 512, 0, stream>>>(Xt, xin, xa, xn);
    k_gemm<256, 128, 4, 2, 1, 3, 0, D_, 2 * F_, 0, 1><<<(S_ / 256) * (2 * F_ / 128), 512, 0, stream>>>(xn, w13t, h, nullptr);
    u16* dst = (l == 0) ? xb : out;
    k_gemm<64, 80, 4, 1, 2, 4, 1, F_, D_><<<(S_ / 64) * (D_ / 80), 512, 0, stream>>>(h, w2t, dst, xa);
    xin = xb;
  }
}
```
